```python
import torch

def _softmax(x, axis=-1):
    return torch.softmax(x, dim=axis)
def _gelu(x):
    return torch.nn.functional.gelu(x, approximate="tanh")
def _silu(x):
    return torch.nn.functional.silu(x)
def _sigmoid(x):
    return torch.sigmoid(x)
def _layernorm(x, axis=-1, eps=1e-5):
    m = x.mean(dim=axis, keepdim=True)
    v = x.var(dim=axis, keepdim=True, unbiased=False)
    return (x - m) / torch.sqrt(v + eps)
def _scatter_add(tgt, idx, src, axis=0):
    return tgt.clone().index_add_(axis, idx, src)
def _segment_sum(src, seg, n):
    out = torch.zeros((n,) + src.shape[1:], dtype=src.dtype)
    return out.index_add_(0, seg, src)
def _pad(x, pw):
    flat = tuple(v for pair in reversed(pw) for v in pair)
    return torch.nn.functional.pad(x, flat)

kernel_name = 'interleave_layers__linear_attn_block__sliding_block__prompt'

def setup_inputs():

    _w = (lambda a: (lambda t: t.to(torch.bfloat16) if t.is_floating_point() else t)(torch.as_tensor(a)))
    def _A_setup():
        B, S, D = 1, 2048, 1024
        return {
            "x": _w((lambda *s: torch.randn(*s, dtype=torch.bfloat16) * 0.05)(B, S, D)),
            "wqkv": _w((lambda *s: torch.randn(*s, dtype=torch.bfloat16) * 0.05)(D, 3 * D)),
            "wo": _w((lambda *s: torch.randn(*s, dtype=torch.bfloat16) * 0.05)(D, D)),
        }
    _A_in = _A_setup()

    def _B_setup():
        B, S, D = 1, 2048, 1024
        return {
            "x": _w((lambda *s: torch.randn(*s, dtype=torch.bfloat16) * 0.05)(B, S, D)),
            "wqkv": _w((lambda *s: torch.randn(*s, dtype=torch.bfloat16) * 0.05)(D, 3 * D)),
            "wo": _w((lambda *s: torch.randn(*s, dtype=torch.bfloat16) * 0.05)(D, D)),
        }
    _B_in = _B_setup()

    return {
        "A_x": _A_in["x"],
        "A_wqkv": _A_in["wqkv"],
        "A_wo": _A_in["wo"],
        "B_x": _B_in["x"],
        "B_wqkv": _B_in["wqkv"],
        "B_wo": _B_in["wo"],
    }

def reference(A_x, A_wqkv, A_wo, B_x, B_wqkv, B_wo):
    def _A_ref(x, wqkv, wo):
        B, S, D = x.shape
        H = 32; Dh = D // H
        qkv = (lambda x, *d: x.permute(*d))((x @ wqkv).reshape(B, S, 3, H, Dh), 2, 0, 3, 1, 4)
        q, k, v = qkv[0], qkv[1], qkv[2]
        phi = lambda t: torch.where(t > 0, t + 1, torch.exp(t)) + 1e-6
        pq, pk = phi(q), phi(k)
        kv = torch.einsum("bhsd,bhse->bhde", pk, v)
        z = torch.einsum("bhsd->bhd", pk)
        num = torch.einsum("bhqd,bhde->bhqe", pq, kv)
        den = torch.einsum("bhqd,bhd->bhq", pq, z)[..., None] + 1e-6
        o = num / den
        return (lambda x, *d: x.permute(*d))(o, 0, 2, 1, 3).reshape(B, S, D) @ wo

    def _B_ref(x, wqkv, wo):
        B, S, D = x.shape
        H = 32; Dh = D // H; W = 256
        qkv = (lambda x, *d: x.permute(*d))((x @ wqkv).reshape(B, S, 3, H, Dh), 2, 0, 3, 1, 4)
        q, k, v = qkv[0], qkv[1], qkv[2]
        scores = torch.einsum("bhqd,bhkd->bhqk", q, k) * (Dh ** -0.5)
        idx = torch.arange(S)
        mask = (idx[:, None] - idx[None, :] >= 0) & (idx[:, None] - idx[None, :] < W)
        scores = torch.where(mask[None, None], scores, -1e9)
        p = _softmax(scores, axis=-1)
        o = torch.einsum("bhqk,bhkd->bhqd", p, v)
        return (lambda x, *d: x.permute(*d))(o, 0, 2, 1, 3).reshape(B, S, D) @ wo

    x = A_x
    x = x + _A_ref(_layernorm(x, axis=-1), A_wqkv, A_wo)
    x = x + _B_ref(_layernorm(x, axis=-1), B_wqkv, B_wo)
    x = x + _A_ref(_layernorm(x, axis=-1), A_wqkv, A_wo)
    x = x + _B_ref(_layernorm(x, axis=-1), B_wqkv, B_wo)
    return x
```

```cpp
#include <hip/hip_runtime.h>
#include <hip/hip_bf16.h>
#include <stdint.h>

constexpr int S_ = 2048, D_ = 1024, H_ = 32, DH = 32, WIN = 256;
constexpr int N3 = 3 * D_;
constexpr int NPART = 8;
constexpr int CSP = 8;

typedef __attribute__((ext_vector_type(8))) short bf16x8;
typedef __attribute__((ext_vector_type(4))) short s16x4;
typedef __attribute__((ext_vector_type(4))) float f32x4;
typedef __attribute__((ext_vector_type(16))) float f32x16;
typedef __attribute__((ext_vector_type(4))) unsigned u32x4;
typedef __attribute__((ext_vector_type(2))) unsigned u32x2;
typedef __attribute__((address_space(3))) s16x4* lds_s16x4_p;
typedef unsigned short u16;

#define DEV __device__ __forceinline__

DEV float bf2f(u16 v) { return __uint_as_float(((unsigned)v) << 16); }
DEV u16 f2bf(float f) {
  unsigned u = __float_as_uint(f);
  u += 0x7fffu + ((u >> 16) & 1u);
  return (u16)(u >> 16);
}
DEV unsigned pack2(float lo, float hi) { return (unsigned)f2bf(lo) | ((unsigned)f2bf(hi) << 16); }
DEV float bfr(float f) { return bf2f(f2bf(f)); }
DEV float phi_f(float t) { return (t > 0.f ? t + 1.f : __expf(t)) + 1e-6f; }

DEV s16x4 tr_read(const char* p) {
  return __builtin_amdgcn_ds_read_tr16_b64_v4i16((lds_s16x4_p)(p));
}
DEV bf16x8 cat8(s16x4 a, s16x4 b) { return __builtin_shufflevector(a, b, 0, 1, 2, 3, 4, 5, 6, 7); }

#define WAITV(n) asm volatile("s_waitcnt vmcnt(" #n ")" ::: "memory")
template <int N> DEV void waitv() { asm volatile("s_waitcnt vmcnt(%0)" ::"n"(N) : "memory"); }
DEV void raw_barrier() {
  asm volatile("s_waitcnt lgkmcnt(0)" ::: "memory");
  __builtin_amdgcn_s_barrier();
  asm volatile("" ::: "memory");
}

template <int BM, int NSEG, int WGM, int WGN, int NSTAGE>
struct GemmCfg {
  static constexpr int NW = WGM * WGN;
  static constexpr int NT_ = NW * 64;
  static constexpr int BN = NSEG * 64;
  static constexpr int WTM = BM / WGM, WTN = BN / WGN;
  static constexpr int MB = WTM / 16, NB = WTN / 16;
  static constexpr int APIECES = BM / 8, BPIECES = NSEG * 8;
  static constexpr int PPW = (APIECES + BPIECES) / NW;
  static constexpr int STAGE_BYTES = (APIECES + BPIECES) * 1024;
  static constexpr int LDS_BYTES = STAGE_BYTES * NSTAGE;
  static_assert((APIECES + BPIECES) % NW == 0, "pieces");
};

template <int BM, int NSEG, int WGM, int WGN, int NSTAGE>
DEV void gemm_mainloop(const u16* __restrict__ A, int lda, const u16* __restrict__ B, int ldb,
                       int m0, const int* colbase  , char* smem,
                       f32x4 (&acc)[GemmCfg<BM, NSEG, WGM, WGN, NSTAGE>::MB][GemmCfg<BM, NSEG, WGM, WGN, NSTAGE>::NB]) {
  using C = GemmCfg<BM, NSEG, WGM, WGN, NSTAGE>;
  constexpr int KT = D_ / 64;
  const int tid = threadIdx.x, lane = tid & 63;
  const int wid = __builtin_amdgcn_readfirstlane(tid >> 6);
  const int wm = wid / WGN, wn = wid % WGN;

  const u16* gp[C::PPW];
  int gstep[C::PPW];
#pragma unroll
  for (int i = 0; i < C::PPW; ++i) {
    int p = wid * C::PPW + i;
    if (p < C::APIECES) {
      int row = p * 8 + (lane >> 3), slot = lane & 7;
      int chunk = slot ^ ((row >> 1) & 7);
      gp[i] = A + (long)(m0 + row) * lda + chunk * 8;
      gstep[i] = 64;
    } else {
      int pb = p - C::APIECES;
      int seg = pb >> 3, k = (pb & 7) * 8 + (lane >> 3), slot = lane & 7;
      int f = ((k >> 1) & 1) | (((k >> 3) & 1) << 1);
      int chunk = slot ^ (f << 1);
      int cb = colbase[0];
#pragma unroll
      for (int s = 1; s < NSEG; ++s) if (seg == s) cb = colbase[s];
      gp[i] = B + (long)k * ldb + cb + chunk * 8;
      gstep[i] = 64 * ldb;
    }
  }
  auto issue = [&](int stage) {
    char* base = smem + stage * C::STAGE_BYTES + wid * C::PPW * 1024;
#pragma unroll
    for (int i = 0; i < C::PPW; ++i) {
      __builtin_amdgcn_global_load_lds((const unsigned*)gp[i], (unsigned*)(base + i * 1024), 16, 0, 0);
      gp[i] += gstep[i];
    }
  };

  const int l15 = lane & 15, g = lane >> 4;
  const int a_off = (wm * C::WTM + l15) * 128;
  const int a_sw = (l15 >> 1);
  const int q = (lane & 15) >> 2, p = lane & 3;
  const int fB = ((q >> 1) & 1) | ((g & 1) << 1);
  const int b_base = C::APIECES * 1024 + (8 * g + q) * 128 + (p & 1) * 8;

#pragma unroll
  for (int s = 0; s < NSTAGE - 1; ++s) issue(s);

  auto compute = [&](int stage) {
    const char* sb = smem + stage * C::STAGE_BYTES;
#pragma unroll
    for (int ks = 0; ks < 2; ++ks) {
      bf16x8 af[C::MB];
#pragma unroll
      for (int mb = 0; mb < C::MB; ++mb)
        af[mb] = *(const bf16x8*)(sb + a_off + mb * 2048 + (((ks * 4 + g) ^ a_sw) << 4));
#pragma unroll
      for (int nb = 0; nb < C::NB; ++nb) {
        int gnb = wn * C::NB + nb;
        int seg = gnb >> 2, c = gnb & 3;
        const char* bp = sb + b_base + seg * 8192 + ks * 4096 + ((((2 * c + (p >> 1)) ^ (fB << 1))) << 4);
        bf16x8 bfrag = cat8(tr_read(bp), tr_read(bp + 512));
#pragma unroll
        for (int mb = 0; mb < C::MB; ++mb)
          acc[mb][nb] = __builtin_amdgcn_mfma_f32_16x16x32_bf16(af[mb], bfrag, acc[mb][nb], 0, 0, 0);
      }
    }
  };

  int cs = 0, is = NSTAGE - 1;
#pragma unroll 1
  for (int kt = 0; kt < KT - (NSTAGE - 2); ++kt) {
    waitv<C::PPW*(NSTAGE - 2)>();
    raw_barrier();
    if (kt + NSTAGE - 1 < KT) issue(is);
    compute(cs);
    cs = (cs + 1 == NSTAGE) ? 0 : cs + 1;
    is = (is + 1 == NSTAGE) ? 0 : is + 1;
  }
#pragma unroll 1
  for (int kt = KT - (NSTAGE - 2); kt < KT; ++kt) {
    waitv<0>();
    raw_barrier();
    compute(cs);
    cs = (cs + 1 == NSTAGE) ? 0 : cs + 1;
  }
  raw_barrier();
}

__global__ __launch_bounds__(256) void k_init(const u16* __restrict__ x, float2* __restrict__ stats,
                                              const u16* __restrict__ wA, const u16* __restrict__ wB,
                                              float* __restrict__ csA, float* __restrict__ csB) {
  const int tid = threadIdx.x;
  if (blockIdx.x < S_ / 4) {
    int row = blockIdx.x * 4 + (tid >> 6), lane = tid & 63;
    const bf16x8* px = (const bf16x8*)(x + (long)row * D_ + lane * 16);
    bf16x8 v0 = px[0], v1 = px[1];
    float f[16];
#pragma unroll
    for (int j = 0; j < 8; ++j) { f[j] = bf2f((u16)v0[j]); f[8 + j] = bf2f((u16)v1[j]); }
    float s = 0;
#pragma unroll
    for (int j = 0; j < 16; ++j) s += f[j];
    s += __shfl_xor(s, 1); s += __shfl_xor(s, 2); s += __shfl_xor(s, 4);
    float mu = s * (1.f / 128.f), m2 = 0;
#pragma unroll
    for (int j = 0; j < 16; ++j) { float d = f[j] - mu; m2 += d * d; }
    m2 += __shfl_xor(m2, 1); m2 += __shfl_xor(m2, 2); m2 += __shfl_xor(m2, 4);
    if ((lane & 7) == 0) stats[row * NPART + (lane >> 3)] = make_float2(s, m2);
  } else {
    int b = blockIdx.x - S_ / 4;
    int wsel = b / (12 * CSP); b %= 12 * CSP;
    int cb = b / CSP, rs = b % CSP;
    const u16* w = wsel ? wB : wA;
    float* cs = wsel ? csB : csA;
    __shared__ float red[8][256];
    int cg = tid & 31, rg = tid >> 5;
    float a[8] = {0, 0, 0, 0, 0, 0, 0, 0};
    for (int r = rg; r < 128; r += 8) {
      bf16x8 v = *(const bf16x8*)(w + (long)(rs * 128 + r) * N3 + cb * 256 + cg * 8);
#pragma unroll
      for (int j = 0; j < 8; ++j) a[j] += bf2f((u16)v[j]);
    }
#pragma unroll
    for (int j = 0; j < 8; ++j) red[rg][cg * 8 + j] = a[j];
    __syncthreads();
    float t = 0;
#pragma unroll
    for (int i = 0; i < 8; ++i) t += red[i][tid];
    cs[rs * N3 + cb * 256 + tid] = t;
  }
}

using G1C = GemmCfg<128, 3, 2, 4, 3>;
constexpr int G1_TP = 200;

template <int MODE>
__global__ __launch_bounds__(512, 2) void k_qkv(const u16* __restrict__ x, const u16* __restrict__ w,
                                                const float2* __restrict__ stats, const float* __restrict__ csp,
                                                u16* __restrict__ out  , float* __restrict__ kvp,
                                                float* __restrict__ zp) {
  __shared__ __attribute__((aligned(16))) char smem[G1C::LDS_BYTES + 2048];
  float* s_mean = (float*)(smem + G1C::LDS_BYTES);
  float* s_rstd = s_mean + 128;
  float* s_cs = s_rstd + 128;
  const int tid = threadIdx.x, lane = tid & 63;
  int bid = blockIdx.x;
  int xcd = bid & 7, idx = bid >> 3;
  int mblk = (xcd & 1) * 8 + (idx & 7), nblk = (xcd >> 1) * 4 + (idx >> 3);
  const int m0 = mblk * 128;
  int colbase[3] = {nblk * 64, D_ + nblk * 64, 2 * D_ + nblk * 64};

  if (tid < 128) {
    const float2* sp = stats + (long)(m0 + tid) * NPART;
    float2 pr[NPART];
#pragma unroll
    for (int i = 0; i < NPART; ++i) pr[i] = sp[i];
    float s = 0;
#pragma unroll
    for (int i = 0; i < NPART; ++i) s += pr[i].x;
    float mean = s * (1.f / D_), m2 = 0;
#pragma unroll
    for (int i = 0; i < NPART; ++i) { float d = pr[i].x * (1.f / 128.f) - mean; m2 += pr[i].y + 128.f * d * d; }
    s_mean[tid] = mean;
    s_rstd[tid] = rsqrtf(m2 * (1.f / D_) + 1e-5f);
  } else if (tid < 128 + 192) {
    int c = tid - 128;
    int col = colbase[c >> 6] + (c & 63);
    float t = 0;
#pragma unroll
    for (int i = 0; i < CSP; ++i) t += csp[i * N3 + col];
    s_cs[c] = t;
  }

  f32x4 acc[G1C::MB][G1C::NB];
#pragma unroll
  for (int i = 0; i < G1C::MB; ++i)
#pragma unroll
    for (int j = 0; j < G1C::NB; ++j) acc[i][j] = (f32x4){0, 0, 0, 0};

  gemm_mainloop<128, 3, 2, 4, 3>(x, D_, w, N3, m0, colbase, smem, acc);

  const int wid = tid >> 6, wm = wid / 4, wn = wid % 4;
  u16* T = (u16*)smem;
  const int l15 = lane & 15, g = lane >> 4;
#pragma unroll
  for (int nb = 0; nb < G1C::NB; ++nb) {
    int col = wn * G1C::WTN + nb * 16 + l15;
    float csv = s_cs[col];
    int seg = col >> 6;
#pragma unroll
    for (int mb = 0; mb < G1C::MB; ++mb) {
#pragma unroll
      for (int r = 0; r < 4; ++r) {
        int row = wm * G1C::WTM + mb * 16 + g * 4 + r;
        float y = (acc[mb][nb][r] - s_mean[row] * csv) * s_rstd[row];
        y = bfr(y);
        if (MODE == 0 && seg < 2) y = phi_f(y);
        T[row * G1_TP + col] = f2bf(y);
      }
    }
  }
  __syncthreads();
  if (MODE == 1) {
#pragma unroll
    for (int i = 0; i < 6; ++i) {
      int ch = tid + i * 512;
      int row = ch / 24, c = ch % 24;
      u32x4 v = *(const u32x4*)(T + row * G1_TP + c * 8);
      int seg = c >> 3;
      *(u32x4*)(out + (long)(m0 + row) * N3 + seg * D_ + nblk * 64 + (c & 7) * 8) = v;
    }
  } else {
#pragma unroll
    for (int i = 0; i < 2; ++i) {
      int ch = tid + i * 512;
      int row = ch >> 3, c = ch & 7;
      u32x4 v = *(const u32x4*)(T + row * G1_TP + c * 8);
      *(u32x4*)(out + (long)(m0 + row) * D_ + nblk * 64 + c * 8) = v;
    }
    int hh = wid >> 2, dt = (wid >> 1) & 1, et = wid & 1;
    int q = (lane & 15) >> 2, p = lane & 3;
    f32x4 kv = {0, 0, 0, 0}, zz = {0, 0, 0, 0};
    bf16x8 ones;
#pragma unroll
    for (int j = 0; j < 8; ++j) ones[j] = (short)0x3F80;
#pragma unroll
    for (int ks = 0; ks < 4; ++ks) {
      int r0 = ks * 32 + 8 * g + q;
      const char* pa = (const char*)(T + r0 * G1_TP + 64 + hh * 32 + dt * 16 + 4 * p);
      const char* pb = (const char*)(T + r0 * G1_TP + 128 + hh * 32 + et * 16 + 4 * p);
      bf16x8 a = cat8(tr_read(pa), tr_read(pa + 4 * G1_TP * 2));
      bf16x8 b = cat8(tr_read(pb), tr_read(pb + 4 * G1_TP * 2));
      kv = __builtin_amdgcn_mfma_f32_16x16x32_bf16(a, b, kv, 0, 0, 0);
      zz = __builtin_amdgcn_mfma_f32_16x16x32_bf16(a, ones, zz, 0, 0, 0);
    }
    int head = nblk * 2 + hh;
    float* kp = kvp + ((long)(mblk * H_ + head) * 32) * 32;
#pragma unroll
    for (int r = 0; r < 4; ++r) {
      int d = dt * 16 + g * 4 + r;
      kp[d * 32 + et * 16 + l15] = kv[r];
      if (et == 0 && l15 == 0) zp[mblk * D_ + head * 32 + d] = zz[r];
    }
  }
}

__global__ __launch_bounds__(256) void k_lin_mid(const float* __restrict__ kvp, const float* __restrict__ zp,
                                                 const u16* __restrict__ wo, u16* __restrict__ M,
                                                 u16* __restrict__ pq  ) {
  __shared__ float sh[1024 + 32];
  const int tid = threadIdx.x;
  if (blockIdx.x < 128) {
    int h = blockIdx.x >> 2, nc = blockIdx.x & 3;
    {
      f32x4 s = {0, 0, 0, 0};
#pragma unroll
      for (int mb = 0; mb < 16; ++mb) s += *(const f32x4*)(kvp + ((long)(mb * H_ + h) * 1024) + tid * 4);
      *(f32x4*)(sh + tid * 4) = s;
    }
    __syncthreads();
    int n = nc * 256 + tid;
    float a[32];
#pragma unroll
    for (int d = 0; d < 32; ++d) a[d] = 0;
    for (int e = 0; e < 32; ++e) {
      float wv = bf2f(wo[(long)(h * 32 + e) * D_ + n]);
#pragma unroll
      for (int d = 0; d < 32; ++d) a[d] += sh[d * 32 + e] * wv;
    }
#pragma unroll
    for (int d = 0; d < 32; ++d) M[(long)(h * 32 + d) * D_ + n] = f2bf(a[d]);
  } else {
    int b = blockIdx.x - 128;
    {
      f32x4 s = {0, 0, 0, 0};
#pragma unroll
      for (int mb = 0; mb < 16; ++mb) s += *(const f32x4*)(zp + mb * D_ + tid * 4);
      *(f32x4*)(sh + tid * 4) = s;
    }
    __syncthreads();
    int row = b * 8 + (tid >> 5), h = tid & 31;
    u16* pp = pq + (long)row * D_ + h * 32;
    bf16x8 v[4];
#pragma unroll
    for (int i = 0; i < 4; ++i) v[i] = *(const bf16x8*)(pp + i * 8);
    float den = 0;
#pragma unroll
    for (int i = 0; i < 4; ++i)
#pragma unroll
      for (int j = 0; j < 8; ++j) den += bf2f((u16)v[i][j]) * sh[h * 32 + i * 8 + j];
    den += 1e-6f;
    float inv = 1.f / den;
#pragma unroll
    for (int i = 0; i < 4; ++i) {
      u32x4 o;
#pragma unroll
      for (int j = 0; j < 4; ++j)
        o[j] = pack2(bf2f((u16)v[i][2 * j]) * inv, bf2f((u16)v[i][2 * j + 1]) * inv);
      *(u32x4*)(pp + i * 8) = o;
    }
  }
}

DEV int crow(int r, int hi) { return (r & 3) + 8 * (r >> 2) + 4 * hi; }

__global__ __launch_bounds__(256) void k_swa(const u16* __restrict__ qkv, u16* __restrict__ att) {
  __shared__ __attribute__((aligned(16))) char sK[384 * 64];
  __shared__ __attribute__((aligned(16))) char sV[384 * 64];
  const int tid = threadIdx.x, lane = tid & 63, wid = tid >> 6;
  const int h = blockIdx.x & 31, qb = blockIdx.x >> 5;
  const int q0 = qb * 128, kbase = q0 - 256;
#pragma unroll
  for (int i = 0; i < 6; ++i) {
    int ch = tid + i * 256;
    int r = ch >> 2, c = ch & 3;
    int key = kbase + r;
    u32x4 kv4 = {0, 0, 0, 0}, vv4 = {0, 0, 0, 0};
    if (key >= 0) {
      kv4 = *(const u32x4*)(qkv + (long)key * N3 + D_ + h * 32 + c * 8);
      vv4 = *(const u32x4*)(qkv + (long)key * N3 + 2 * D_ + h * 32 + c * 8);
    }
    *(u32x4*)(sK + r * 64 + ((c ^ ((r >> 2) & 3)) << 4)) = kv4;
    *(u32x4*)(sV + r * 64 + (c << 4)) =vv4;
  }
  const int l31 = lane & 31, hi = lane >> 5;
  const int qw = q0 + wid * 32;
  const int qi = qw + l31;
  bf16x8 qf[2];
#pragma unroll
  for (int ks = 0; ks < 2; ++ks) qf[ks] = *(const bf16x8*)(qkv + (long)qi * N3 + h * 32 + ks * 16 + hi * 8);
  __syncthreads();

  f32x16 sc[9];
#pragma unroll
  for (int kt = 0; kt < 9; ++kt) {
    int r = wid * 32 + kt * 32 + l31;
    f32x16 a16;
#pragma unroll
    for (int i = 0; i < 16; ++i) a16[i] = 0;
#pragma unroll
    for (int ks = 0; ks < 2; ++ks) {
      int c = ks * 2 + hi;
      bf16x8 kf = *(const bf16x8*)(sK + r * 64 + ((c ^ ((r >> 2) & 3)) << 4));
      a16 = __builtin_amdgcn_mfma_f32_32x32x16_bf16(kf, qf[ks], a16, 0, 0, 0);
    }
    sc[kt] = a16;
  }
  const float scale_l2 = 0.17677669529663687f * 1.4426950408889634f;
  float mx = -3.0e38f;
#pragma unroll
  for (int kt = 0; kt < 9; ++kt)
#pragma unroll
    for (int r = 0; r < 16; ++r) {
      int key = qw - 256 + kt * 32 + crow(r, hi);
      int dlt = qi - key;
      bool ok = (dlt >= 0) && (dlt < WIN) && (key >= 0);
      float v = ok ? sc[kt][r] * scale_l2 : -3.0e38f;
      sc[kt][r] = v;
      mx = fmaxf(mx, v);
    }
  mx = fmaxf(mx, __shfl_xor(mx, 32));
  float sum = 0;
#pragma unroll
  for (int kt = 0; kt < 9; ++kt)
#pragma unroll
    for (int r = 0; r < 16; ++r) {
      float pv = __builtin_amdgcn_exp2f(sc[kt][r] - mx);
      sc[kt][r] = pv;
      sum += pv;
    }
  sum += __shfl_xor(sum, 32);
  f32x16 o;
#pragma unroll
  for (int i = 0; i < 16; ++i) o[i] = 0;
  const int q4 = (lane & 15) >> 2, p4 = lane & 3, blk = (lane >> 4) & 1;
#pragma unroll
  for (int kt = 0; kt < 9; ++kt) {
#pragma unroll
    for (int s = 0; s < 2; ++s) {
      u32x4 pk;
#pragma unroll
      for (int j = 0; j < 4; ++j) pk[j] = pack2(sc[kt][8 * s + 2 * j], sc[kt][8 * s + 2 * j + 1]);
      bf16x8 pf = __builtin_bit_cast(bf16x8, pk);
      int r0 = wid * 32 + kt * 32 + 16 * s + 4 * hi + q4;
      const char* vp = sV + r0 * 64 + (16 * blk + 4 * p4) * 2;
      bf16x8 vf = cat8(tr_read(vp), tr_read(vp + 8 * 64));
      o = __builtin_amdgcn_mfma_f32_32x32x16_bf16(vf, pf, o, 0, 0, 0);
    }
  }
  float inv = 1.f / sum;
  u16* op = att + (long)qi * D_ + h * 32;
#pragma unroll
  for (int gq = 0; gq < 4; ++gq) {
    u32x2 w2;
    w2[0] = pack2(o[4 * gq + 0] * inv, o[4 * gq + 1] * inv);
    w2[1] = pack2(o[4 * gq + 2] * inv, o[4 * gq + 3] * inv);
    *(u32x2*)(op + 8 * gq + 4 * hi) = w2;
  }
}

using G2C = GemmCfg<64, 2, 2, 2, 4>;

__global__ __launch_bounds__(256, 1) void k_out(const u16* __restrict__ a, const u16* __restrict__ w,
                                                const u16* __restrict__ xold, u16* __restrict__ xnew,
                                                float2* __restrict__ stats) {
  __shared__ __attribute__((aligned(16))) char smem[G2C::LDS_BYTES];
  const int tid = threadIdx.x, lane = tid & 63;
  int bid = blockIdx.x;
  int xcd = bid & 7, idx = bid >> 3;
  int mblk = xcd * 4 + (idx & 3), nblk = idx >> 2;
  const int m0 = mblk * 64;
  int colbase[2] = {nblk * 128, nblk * 128 + 64};
  f32x4 acc[G2C::MB][G2C::NB];
#pragma unroll
  for (int i = 0; i < G2C::MB; ++i)
#pragma unroll
    for (int j = 0; j < G2C::NB; ++j) acc[i][j] = (f32x4){0, 0, 0, 0};
  gemm_mainloop<64, 2, 2, 2, 4>(a, D_, w, D_, m0, colbase, smem, acc);

  const int wid = tid >> 6, wm = wid / 2, wn = wid % 2;
  float* T = (float*)smem;
  constexpr int TP = 132;
  const int l15 = lane & 15, g = lane >> 4;
#pragma unroll
  for (int mb = 0; mb < G2C::MB; ++mb)
#pragma unroll
    for (int nb = 0; nb < G2C::NB; ++nb)
#pragma unroll
      for (int r = 0; r < 4; ++r)
        T[(wm * 32 + mb * 16 + g * 4 + r) * TP + wn * 64 + nb * 16 + l15] = acc[mb][nb][r];
  __syncthreads();
  int row = tid >> 2, part = tid & 3;
  const u16* xo = xold + (long)(m0 + row) * D_ + nblk * 128 + part * 32;
  u16* xn = xnew + (long)(m0 + row) * D_ + nblk * 128 + part * 32;
  float v[32];
  float s = 0;
#pragma unroll
  for (int i = 0; i < 4; ++i) {
    bf16x8 xv = *(const bf16x8*)(xo + i * 8);
    u32x4 o;
#pragma unroll
    for (int j = 0; j < 4; ++j) {
      float f0 = bfr(bf2f((u16)xv[2 * j]) + T[row * TP + part * 32 + i * 8 + 2 * j]);
      float f1 = bfr(bf2f((u16)xv[2 * j + 1]) + T[row * TP + part * 32 + i * 8 + 2 * j + 1]);
      v[i * 8 + 2 * j] = f0; v[i * 8 + 2 * j + 1] = f1;
      s += f0 + f1;
      o[j] = pack2(f0, f1);
    }
    *(u32x4*)(xn + i * 8) = o;
  }
  s += __shfl_xor(s, 1); s += __shfl_xor(s, 2);
  float mu = s * (1.f / 128.f), m2 = 0;
#pragma unroll
  for (int j = 0; j < 32; ++j) { float d = v[j] - mu; m2 += d * d; }
  m2 += __shfl_xor(m2, 1); m2 += __shfl_xor(m2, 2);
  if (part == 0) stats[(long)(m0 + row) * NPART + nblk] = make_float2(s, m2);
}

extern "C" void kernel_launch(void* const* d_in, const int* in_sizes, int n_in, void* d_out, int out_size,
                              void* d_ws, size_t ws_size, hipStream_t stream) {
  const u16* x_in = (const u16*)d_in[0];
  const u16* A_wqkv = (const u16*)d_in[1];
  const u16* A_wo = (const u16*)d_in[2];
  const u16* B_wqkv = (const u16*)d_in[4];
  const u16* B_wo = (const u16*)d_in[5];
  u16* out = (u16*)d_out;
  char* ws = (char*)d_ws;
  size_t off = 0;
  auto take = [&](size_t bytes) { char* p = ws + off; off += (bytes + 255) & ~(size_t)255; return p; };
  u16* xb0 = (u16*)take((size_t)S_ * D_ * 2);
  u16* xb1 = (u16*)take((size_t)S_ * D_ * 2);
  u16* qkv = (u16*)take((size_t)S_ * N3 * 2);
  u16* att = (u16*)take((size_t)S_ * D_ * 2);
  u16* Mm = (u16*)take((size_t)D_ * D_ * 2);
  float* kvp = (float*)take((size_t)16 * H_ * 1024 * 4);
  float* zp = (float*)take((size_t)16 * D_ * 4);
  float2* stats = (float2*)take((size_t)S_ * NPART * 8);
  float* csA = (float*)take((size_t)CSP * N3 * 4);
  float* csB = (float*)take((size_t)CSP * N3 * 4);

  k_init<<<S_ / 4 + 2 * 12 * CSP, 256, 0, stream>>>(x_in, stats, A_wqkv, B_wqkv, csA, csB);
  k_qkv<0><<<256, 512, 0, stream>>>(x_in, A_wqkv, stats, csA, att, kvp, zp);
  k_lin_mid<<<384, 256, 0, stream>>>(kvp, zp, A_wo, Mm, att);
  k_out<<<256, 256, 0, stream>>>(att, Mm, x_in, xb0, stats);
  k_qkv<1><<<256, 512, 0, stream>>>(xb0, B_wqkv, stats, csB, qkv, kvp, zp);
  k_swa<<<512, 256, 0, stream>>>(qkv, att);
  k_out<<<256, 256, 0, stream>>>(att, B_wo, xb0, xb1, stats);
  k_qkv<0><<<256, 512, 0, stream>>>(xb1, A_wqkv, stats, csA, att, kvp, zp);
  k_lin_mid<<<384, 256, 0, stream>>>(kvp, zp, A_wo, Mm, att);
  k_out<<<256, 256, 0, stream>>>(att, Mm, xb1, xb0, stats);
  k_qkv<1><<<256, 512, 0, stream>>>(xb0, B_wqkv, stats, csB, qkv, kvp, zp);
  k_swa<<<512, 256, 0, stream>>>(qkv, att);
  k_out<<<256, 256, 0, stream>>>(att, B_wo, xb0, out, stats);
}
```

```cpp
#include <hip/hip_runtime.h>
#include <hip/hip_bf16.h>
#include <stdint.h>
#include <type_traits>

constexpr int S_ = 2048, D_ = 1024, H_ = 32, DH = 32, WIN = 256;
constexpr int N3 = 3 * D_;
constexpr int NPART = 8;
constexpr int CSP = 8;

typedef __attribute__((ext_vector_type(8))) short bf16x8;
typedef __attribute__((ext_vector_type(4))) short s16x4;
typedef __attribute__((ext_vector_type(4))) float f32x4;
typedef __attribute__((ext_vector_type(16))) float f32x16;
typedef __attribute__((ext_vector_type(4))) unsigned u32x4;
typedef __attribute__((ext_vector_type(2))) unsigned u32x2;
typedef __attribute__((address_space(3))) s16x4* lds_s16x4_p;
typedef unsigned short u16;

#define DEV __device__ __forceinline__

DEV float bf2f(u16 v) { return __uint_as_float(((unsigned)v) << 16); }
DEV unsigned pack2(float lo, float hi) {
  unsigned r;
  asm("v_cvt_pk_bf16_f32 %0, %1, %2" : "=v"(r) : "v"(lo), "v"(hi));
  return r;
}
DEV u16 f2bf(float f) { return (u16)(pack2(f, f) & 0xffffu); }
DEV float bfr(float f) { return __uint_as_float(pack2(f, f) << 16); }
DEV float phi_f(float t) { return (t > 0.f ? t + 1.f : __expf(t)) + 1e-6f; }

DEV s16x4 tr_read(const char* p) {
  return __builtin_amdgcn_ds_read_tr16_b64_v4i16((lds_s16x4_p)(p));
}
DEV bf16x8 cat8(s16x4 a, s16x4 b) { return __builtin_shufflevector(a, b, 0, 1, 2, 3, 4, 5, 6, 7); }

#define WAITV(n) asm volatile("s_waitcnt vmcnt(" #n ")" ::: "memory")
template <int N> DEV void waitv() { asm volatile("s_waitcnt vmcnt(%0)" ::"n"(N) : "memory"); }
DEV void wait_lgkm0() { __builtin_amdgcn_s_waitcnt(0xC07F); }
DEV void raw_barrier() {
  asm volatile("" ::: "memory");
  wait_lgkm0();
  __builtin_amdgcn_s_barrier();
  asm volatile("" ::: "memory");
}

template <int BM, int NSEG, int WGM, int WGN, int NSTAGE>
struct GemmCfg {
  static constexpr int NW = WGM * WGN;
  static constexpr int NT_ = NW * 64;
  static constexpr int BN = NSEG * 64;
  static constexpr int WTM = BM / WGM, WTN = BN / WGN;
  static constexpr int MB = WTM / 16, NB = WTN / 16;
  static constexpr int APIECES = BM / 8, BPIECES = NSEG * 8;
  static constexpr int PPW = (APIECES + BPIECES) / NW;
  static constexpr int STAGE_BYTES = (APIECES + BPIECES) * 1024;
  static constexpr int LDS_BYTES = STAGE_BYTES * NSTAGE;
  static_assert((APIECES + BPIECES) % NW == 0, "pieces");
};

template <int BM, int NSEG, int WGM, int WGN, int NSTAGE, typename PRE>
DEV void gemm_mainloop(PRE pre, const u16* __restrict__ A, int lda, const u16* __restrict__ B, int ldb,
                       int m0, int colbase0, int segstride, char* smem,
                       f32x4 (&acc)[GemmCfg<BM, NSEG, WGM, WGN, NSTAGE>::MB][GemmCfg<BM, NSEG, WGM, WGN, NSTAGE>::NB]) {
  using C = GemmCfg<BM, NSEG, WGM, WGN, NSTAGE>;
  constexpr int KT = D_ / 64;
  const int tid = threadIdx.x, lane = tid & 63;
  const int wid = __builtin_amdgcn_readfirstlane(tid >> 6);
  const int wm = wid / WGN, wn = wid % WGN;

  const u16* gp[C::PPW];
  int gstep[C::PPW];

#pragma unroll
  for (int i = 0; i < C::PPW; ++i) {
    int p = wid * C::PPW + i;
    if (p < C::APIECES) {
      int row = p * 8 + (lane >> 3), slot = lane & 7;
      int chunk = slot ^ ((row >> 1) & 7);
      gstep[i] = 64;
      gp[i] = A + (long)(m0 + row) * lda + chunk * 8;
    } else {
      int pb = p - C::APIECES;
      int seg = pb >> 3, k = (pb & 7) * 8 + (lane >> 3), slot = lane & 7;
      int f = ((k >> 1) & 1) | (((k >> 3) & 1) << 1);
      int chunk = slot ^ (f << 1);
      int cb = colbase0 + seg * segstride;
      gstep[i] = 64 * ldb;
      gp[i] = B + (long)k * ldb + cb + chunk * 8;
    }
  }
  auto issue_piece = [&](int stage, int i) {
    char* base = smem + stage * C::STAGE_BYTES + wid * C::PPW * 1024;
    __builtin_amdgcn_global_load_lds((const unsigned*)gp[i], (unsigned*)(base + i * 1024), 16, 0, 0);
    gp[i] += gstep[i];
  };

  const int l15 = lane & 15, g = lane >> 4;
  const int a_off = (wm * C::WTM + l15) * 128;
  const int a_sw = (l15 >> 1);
  const int q = (lane & 15) >> 2, p = lane & 3;
  const int fB = ((q >> 1) & 1) | ((g & 1) << 1);
  const int b_base = C::APIECES * 1024 + (8 * g + q) * 128 + (p & 1) * 8;

#pragma unroll
  for (int s = 0; s < NSTAGE; ++s)
#pragma unroll
    for (int i = 0; i < C::PPW; ++i) issue_piece(s, i);
  pre();

  bf16x8 af[2][C::MB];
  s16x4 bl[2][C::NB][2];
  constexpr int NM = C::NB * C::MB;
  constexpr int NRD = C::NB * 2 + C::MB;
  auto read1 = [&](int stage, int ks, int set, int k) {
    const char* sb = smem + stage * C::STAGE_BYTES;
    if (k < C::NB * 2) {
      int nb = k >> 1, hf = k & 1;
      int gnb = wn * C::NB + nb;
      int seg = gnb >> 2, c = gnb & 3;
      const char* bp = sb + b_base + seg * 8192 + ks * 4096 + ((((2 * c + (p >> 1)) ^ (fB << 1))) << 4);
      bl[set][nb][hf] = tr_read(bp + hf * 512);
    } else {
      int mb = k - C::NB * 2;
      af[set][mb] = *(const bf16x8*)(sb + a_off + mb * 2048 + (((ks * 4 + g) ^ a_sw) << 4));
    }
  };
  auto phase = [&](int set, bool rd, int rstage, int rks, bool iss, int dstage) {
    constexpr int NSIDE_MAX = NRD + C::PPW;
    constexpr int PER = (NSIDE_MAX + NM - 1) / NM;
    const int nrd = rd ? NRD : 0;
    const int nside = nrd + (iss ? C::PPW : 0);
#pragma unroll
    for (int nb = 0; nb < C::NB; ++nb)
#pragma unroll
      for (int mb = 0; mb < C::MB; ++mb) {
        const int idx = nb * C::MB + mb;
        acc[mb][nb] = __builtin_amdgcn_mfma_f32_16x16x32_bf16(cat8(bl[set][nb][0], bl[set][nb][1]), af[set][mb], acc[mb][nb], 0, 0, 0);
        __builtin_amdgcn_sched_barrier(0);
#pragma unroll
        for (int e = 0; e < PER; ++e) {
          const int k = idx * PER + e;
          if (k < nrd) read1(rstage, rks, set ^ 1, k);
          else if (k < nside) issue_piece(dstage, k - nrd);
        }
        __builtin_amdgcn_sched_barrier(0);
      }
  };
  const bool grpA = wid < C::NW / 2;

  waitv<C::PPW*(NSTAGE - 1)>();
  raw_barrier();
#pragma unroll
  for (int k = 0; k < NRD; ++k) read1(0, 0, 0, k);
  auto run = [&](auto GA) {
    constexpr bool ga = decltype(GA)::value;
#pragma unroll
    for (int kt = 0; kt < KT; ++kt) {
      const int cs = kt % NSTAGE, ns = (kt + 1) % NSTAGE, ps = (kt + NSTAGE - 1) % NSTAGE;
      __builtin_amdgcn_sched_barrier(0);
      phase(0, true, cs, 1, ga && kt >= 1 && kt - 1 + NSTAGE < KT, ps);
      if (kt + 1 < KT) {
        if (kt + NSTAGE - 1 < KT) waitv<C::PPW*(NSTAGE - 2)>(); else waitv<0>();
        raw_barrier();
        __builtin_amdgcn_sched_barrier(0);
        phase(1, true, ns, 0, !ga && kt + NSTAGE < KT, cs);
      } else {
        phase(1, false, 0, 0, false, 0);
      }
    }
    __builtin_amdgcn_sched_barrier(0);
  };
  if (grpA) run(std::integral_constant<bool, true>{}); else run(std::integral_constant<bool, false>{});
  raw_barrier();
}

__global__ __launch_bounds__(256) void k_init(const u16* __restrict__ x, float2* __restrict__ stats,
                                              const u16* __restrict__ wA, const u16* __restrict__ wB,
                                              float* __restrict__ csA, float* __restrict__ csB) {
  const int tid = threadIdx.x;
  if (blockIdx.x < S_ / 4) {
    int row = blockIdx.x * 4 + (tid >> 6), lane = tid & 63;
    const bf16x8* px = (const bf16x8*)(x + (long)row * D_ + lane * 16);
    bf16x8 v0 = px[0], v1 = px[1];
    float f[16];
#pragma unroll
    for (int j = 0; j < 8; ++j) { f[j] = bf2f((u16)v0[j]); f[8 + j] = bf2f((u16)v1[j]); }
    float s = 0;
#pragma unroll
    for (int j = 0; j < 16; ++j) s += f[j];
    s += __shfl_xor(s, 1); s += __shfl_xor(s, 2); s += __shfl_xor(s, 4);
    float mu = s * (1.f / 128.f), m2 = 0;
#pragma unroll
    for (int j = 0; j < 16; ++j) { float d = f[j] - mu; m2 += d * d; }
    m2 += __shfl_xor(m2, 1); m2 += __shfl_xor(m2, 2); m2 += __shfl_xor(m2, 4);
    if ((lane & 7) == 0) stats[row * NPART + (lane >> 3)] = make_float2(s, m2);
  } else {
    int b = blockIdx.x - S_ / 4;
    int wsel = b / (12 * CSP); b %= 12 * CSP;
    int cb = b / CSP, rs = b % CSP;
    const u16* w = wsel ? wB : wA;
    float* cs = wsel ? csB : csA;
    __shared__ float red[8][256];
    int cg = tid & 31, rg = tid >> 5;
    float a[8] = {0, 0, 0, 0, 0, 0, 0, 0};
    bf16x8 v[16];
#pragma unroll
    for (int i = 0; i < 16; ++i) v[i] = *(const bf16x8*)(w + (long)(rs * 128 + rg + i * 8) * N3 + cb * 256 + cg * 8);
#pragma unroll
    for (int i = 0; i < 16; ++i)
#pragma unroll
      for (int j = 0; j < 8; ++j) a[j] += bf2f((u16)v[i][j]);
#pragma unroll
    for (int j = 0; j < 8; ++j) red[rg][cg * 8 + j] = a[j];
    __syncthreads();
    float t = 0;
#pragma unroll
    for (int i = 0; i < 8; ++i) t += red[i][tid];
    cs[rs * N3 + cb * 256 + tid] = t;
  }
}

using G1C = GemmCfg<128, 3, 2, 4, 3>;
constexpr int G1_TP = 200;

template <int MODE>
__global__ __launch_bounds__(512, 2) void k_qkv(const u16* __restrict__ x, const u16* __restrict__ w,
                                                const float2* __restrict__ stats, const float* __restrict__ csp,
                                                u16* __restrict__ out  , float* __restrict__ kvp,
                                                float* __restrict__ zp) {
  __shared__ __attribute__((aligned(16))) char smem[G1C::LDS_BYTES + 2048];
  float* s_mean = (float*)(smem + G1C::LDS_BYTES);
  float* s_rstd = s_mean + 128;
  float* s_cs = s_rstd + 128;
  const int tid = threadIdx.x, lane = tid & 63;
  int bid = blockIdx.x;
  int xcd = bid & 7, idx = bid >> 3;
  int mblk = (xcd & 1) * 8 + (idx & 7), nblk = (xcd >> 1) * 4 + (idx >> 3);
  const int m0 = mblk * 128;

  f32x4 acc[G1C::MB][G1C::NB];
#pragma unroll
  for (int i = 0; i < G1C::MB; ++i)
#pragma unroll
    for (int j = 0; j < G1C::NB; ++j) acc[i][j] = (f32x4){0, 0, 0, 0};

  auto pre = [&]() {
  if (tid < 128) {
    const float2* sp = stats + (long)(m0 + tid) * NPART;
    float2 pr[NPART];
#pragma unroll
    for (int i = 0; i < NPART; ++i) pr[i] = sp[i];
    float s = 0;
#pragma unroll
    for (int i = 0; i < NPART; ++i) s += pr[i].x;
    float mean = s * (1.f / D_), m2 = 0;
#pragma unroll
    for (int i = 0; i < NPART; ++i) { float d = pr[i].x * (1.f / 128.f) - mean; m2 += pr[i].y + 128.f * d * d; }
    s_mean[tid] = mean;
    s_rstd[tid] = rsqrtf(m2 * (1.f / D_) + 1e-5f);
  } else if (tid < 128 + 192) {
    int c = tid - 128;
    int col = (c >> 6) * D_ + nblk * 64 + (c & 63);
    float t = 0;
#pragma unroll
    for (int i = 0; i < CSP; ++i) t += csp[i * N3 + col];
    s_cs[c] = t;
  }

  };
  gemm_mainloop<128, 3, 2, 4, 3>(pre, x, D_, w, N3, m0, nblk * 64, D_, smem, acc);

  const int wid = tid >> 6, wm = wid / 4, wn = wid % 4;
  u16* T = (u16*)smem;
  const int l15 = lane & 15, g = lane >> 4;
#pragma unroll
  for (int mb = 0; mb < G1C::MB; ++mb) {
    const int row = wm * G1C::WTM + mb * 16 + l15;
    const float mean = s_mean[row], rstd = s_rstd[row];
#pragma unroll
    for (int nb = 0; nb < G1C::NB; ++nb) {
      const int col = wn * G1C::WTN + nb * 16 + g * 4;
      const f32x4 csv = *(const f32x4*)(s_cs + col);
      const int seg = col >> 6;
      float y[4];
#pragma unroll
      for (int r = 0; r < 4; ++r) {
        y[r] = (acc[mb][nb][r] - mean * csv[r]) * rstd;
        if (MODE == 0 && seg < 2) y[r] = phi_f(bfr(y[r]));
      }
      u32x2 w2;
      w2[0] = pack2(y[0], y[1]); w2[1] = pack2(y[2], y[3]);
      *(u32x2*)(T + row * G1_TP + col) = w2;
    }
  }
  __syncthreads();
  if (MODE == 1) {
#pragma unroll
    for (int i = 0; i < 6; ++i) {
      int ch = tid + i * 512;
      int row = ch / 24, c = ch % 24;
      u32x4 v = *(const u32x4*)(T + row * G1_TP + c * 8);
      int seg = c >> 3;
      *(u32x4*)(out + (long)(m0 + row) * N3 + seg * D_ + nblk * 64 + (c & 7) * 8) = v;
    }
  } else {
#pragma unroll
    for (int i = 0; i < 2; ++i) {
      int ch = tid + i * 512;
      int row = ch >> 3, c = ch & 7;
      u32x4 v = *(const u32x4*)(T + row * G1_TP + c * 8);
      *(u32x4*)(out + (long)(m0 + row) * D_ + nblk * 64 + c * 8) = v;
    }
    int hh = wid >> 2, dt = (wid >> 1) & 1, et = wid & 1;
    int q = (lane & 15) >> 2, p = lane & 3;
    f32x4 kv = {0, 0, 0, 0}, zz = {0, 0, 0, 0};
    bf16x8 ones;
#pragma unroll
    for (int j = 0; j < 8; ++j) ones[j] = (short)0x3F80;
#pragma unroll
    for (int ks = 0; ks < 4; ++ks) {
      int r0 = ks * 32 + 8 * g + q;
      const char* pa = (const char*)(T + r0 * G1_TP + 64 + hh * 32 + dt * 16 + 4 * p);
      const char* pb = (const char*)(T + r0 * G1_TP + 128 + hh * 32 + et * 16 + 4 * p);
      bf16x8 a = cat8(tr_read(pa), tr_read(pa + 4 * G1_TP * 2));
      bf16x8 b = cat8(tr_read(pb), tr_read(pb + 4 * G1_TP * 2));
      kv = __builtin_amdgcn_mfma_f32_16x16x32_bf16(a, b, kv, 0, 0, 0);
      zz = __builtin_amdgcn_mfma_f32_16x16x32_bf16(a, ones, zz, 0, 0, 0);
    }
    int head = nblk * 2 + hh;
    float* kp = kvp + ((long)(mblk * H_ + head) * 32) * 32;
#pragma unroll
    for (int r = 0; r < 4; ++r) {
      int d = dt * 16 + g * 4 + r;
      kp[d * 32 + et * 16 + l15] = kv[r];
      if (et == 0 && l15 == 0) zp[mblk * D_ + head * 32 + d] = zz[r];
    }
  }
}

__global__ __launch_bounds__(256) void k_lin_mid(const float* __restrict__ kvp, const float* __restrict__ zp,
                                                 const u16* __restrict__ wo, u16* __restrict__ M,
                                                 u16* __restrict__ pq  ) {
  __shared__ float sh[1024 + 32];
  const int tid = threadIdx.x;
  if (blockIdx.x < 128) {
    int h = blockIdx.x >> 2, nc = blockIdx.x & 3;
    int ng = tid & 31, dg = tid >> 5;
    int n = nc * 256 + ng * 8;
    f32x4 pr[16];
#pragma unroll
    for (int mb = 0; mb < 16; ++mb) pr[mb] = *(const f32x4*)(kvp + ((long)(mb * H_ + h) * 1024) + tid * 4);
    bf16x8 wv[32];
#pragma unroll
    for (int e = 0; e < 32; ++e) wv[e] = *(const bf16x8*)(wo + (long)(h * 32 + e) * D_ + n);
    {
      f32x4 s4 = {0, 0, 0, 0};
#pragma unroll
      for (int mb = 0; mb < 16; ++mb) s4 += pr[mb];
      *(f32x4*)(sh + tid * 4) = s4;
    }
    __syncthreads();
    float a[4][8];
#pragma unroll
    for (int d = 0; d < 4; ++d)
#pragma unroll
      for (int j = 0; j < 8; ++j) a[d][j] = 0;
#pragma unroll
    for (int e = 0; e < 32; ++e) {
      float wf[8];
#pragma unroll
      for (int j = 0; j < 8; ++j) wf[j] = bf2f((u16)wv[e][j]);
#pragma unroll
      for (int d = 0; d < 4; ++d) {
        float kvv = sh[(dg * 4 + d) * 32 + e];
#pragma unroll
        for (int j = 0; j < 8; ++j) a[d][j] += kvv * wf[j];
      }
    }
#pragma unroll
    for (int d = 0; d < 4; ++d) {
      u32x4 o;
#pragma unroll
      for (int j = 0; j < 4; ++j) o[j] = pack2(a[d][2 * j], a[d][2 * j + 1]);
      *(u32x4*)(M + (long)(h * 32 + dg * 4 + d) * D_ + n) = o;
    }
  } else {
    int b = blockIdx.x - 128;
    int row = b * 8 + (tid >> 5), h = tid & 31;
    u16* pp = pq + (long)row * D_ + h * 32;
    f32x4 pr[16];
#pragma unroll
    for (int mb = 0; mb < 16; ++mb) pr[mb] = *(const f32x4*)(zp + mb * D_ + tid * 4);
    bf16x8 v[4];
#pragma unroll
    for (int i = 0; i < 4; ++i) v[i] = *(const bf16x8*)(pp + i * 8);
    {
      f32x4 s4 = {0, 0, 0, 0};
#pragma unroll
      for (int mb = 0; mb < 16; ++mb) s4 += pr[mb];
      *(f32x4*)(sh + tid * 4) = s4;
    }
    __syncthreads();
    float den = 0;
#pragma unroll
    for (int i = 0; i < 4; ++i) {
      f32x4 z0 = *(const f32x4*)(sh + h * 32 + i * 8), z1 = *(const f32x4*)(sh + h * 32 + i * 8 + 4);
#pragma unroll
      for (int j = 0; j < 4; ++j) den += bf2f((u16)v[i][j]) * z0[j] + bf2f((u16)v[i][4 + j]) * z1[j];
    }
    den += 1e-6f;
    float inv = 1.f / den;
#pragma unroll
    for (int i = 0; i < 4; ++i) {
      u32x4 o;
#pragma unroll
      for (int j = 0; j < 4; ++j)
        o[j] = pack2(bf2f((u16)v[i][2 * j]) * inv, bf2f((u16)v[i][2 * j + 1]) * inv);
      *(u32x4*)(pp + i * 8) = o;
    }
  }
}

DEV int crow(int r, int hi) { return (r & 3) + 8 * (r >> 2) + 4 * hi; }

__global__ __launch_bounds__(256, 2) void k_swa(const u16* __restrict__ qkv, u16* __restrict__ att) {
  __shared__ __attribute__((aligned(16))) char sK[384 * 64];
  __shared__ __attribute__((aligned(16))) char sV[384 * 64];
  const int tid = threadIdx.x, lane = tid & 63, wid = tid >> 6;
  const int h = blockIdx.x & 31, qb = blockIdx.x >> 5;
  const int q0 = qb * 128, kbase = q0 - 256;
#pragma unroll
  for (int i = 0; i < 6; ++i) {
    int ch = tid + i * 256;
    int r = ch >> 2, c = ch & 3;
    int key = kbase + r;
    u32x4 kv4 = {0, 0, 0, 0}, vv4 = {0, 0, 0, 0};
    if (key >= 0) {
      kv4 = *(const u32x4*)(qkv + (long)key * N3 + D_ + h * 32 + c * 8);
      vv4 = *(const u32x4*)(qkv + (long)key * N3 + 2 * D_ + h * 32 + c * 8);
    }
    *(u32x4*)(sK + r * 64 + ((c ^ ((r >> 2) & 3)) << 4)) = kv4;
    *(u32x4*)(sV + r * 64 + (c << 4)) =vv4;
  }
  const int l31 = lane & 31, hi = lane >> 5;
  const int qw = q0 + wid * 32;
  const int qi = qw + l31;
  bf16x8 qf[2];
#pragma unroll
  for (int ks = 0; ks < 2; ++ks) qf[ks] = *(const bf16x8*)(qkv + (long)qi * N3 + h * 32 + ks * 16 + hi * 8);
  __syncthreads();

  const int kt0 = (qw >= 256) ? 0 : (256 - qw) / 32;
  f32x16 sc[9];
  const float scale_l2 = 0.17677669529663687f * 1.4426950408889634f;
  float mx = -3.0e38f;
#pragma unroll
  for (int kt = 0; kt < 9; ++kt) {
    f32x16 a16;
#pragma unroll
    for (int i = 0; i < 16; ++i) a16[i] = 0;
    if (kt >= kt0) {
      int r = wid * 32 + kt * 32 + l31;
#pragma unroll
      for (int ks = 0; ks < 2; ++ks) {
        int c = ks * 2 + hi;
        bf16x8 kf = *(const bf16x8*)(sK + r * 64 + ((c ^ ((r >> 2) & 3)) << 4));
        a16 = __builtin_amdgcn_mfma_f32_32x32x16_bf16(kf, qf[ks], a16, 0, 0, 0);
      }
      if (kt == 0) {
#pragma unroll
        for (int r2 = 0; r2 < 16; ++r2) a16[r2] = (crow(r2, hi) > l31) ? a16[r2] : -3.0e38f;
      }
      if (kt == 8) {
#pragma unroll
        for (int r2 = 0; r2 < 16; ++r2) a16[r2] = (crow(r2, hi) <= l31) ? a16[r2] : -3.0e38f;
      }
#pragma unroll
      for (int r2 = 0; r2 < 16; ++r2) mx = fmaxf(mx, a16[r2]);
    } else {
#pragma unroll
      for (int i = 0; i < 16; ++i) a16[i] = -3.0e38f;
    }
    sc[kt] = a16;
  }
  mx = fmaxf(mx, __shfl_xor(mx, 32));
  const float mxs = -mx * scale_l2;
  float sum = 0;
#pragma unroll
  for (int kt = 0; kt < 9; ++kt)
#pragma unroll
    for (int r = 0; r < 16; ++r) {
      float pv = __builtin_amdgcn_exp2f(fmaf(sc[kt][r], scale_l2, mxs));
      sc[kt][r] = pv;
      sum += pv;
    }
  sum += __shfl_xor(sum, 32);
  f32x16 o;
#pragma unroll
  for (int i = 0; i < 16; ++i) o[i] = 0;
  const int q4 = (lane & 15) >> 2, p4 = lane & 3, blk = (lane >> 4) & 1;
#pragma unroll
  for (int kt = 0; kt < 9; ++kt) {
#pragma unroll
    for (int s = 0; s < 2; ++s) {
      u32x4 pk;
#pragma unroll
      for (int j = 0; j < 4; ++j) pk[j] = pack2(sc[kt][8 * s + 2 * j], sc[kt][8 * s + 2 * j + 1]);
      bf16x8 pf = __builtin_bit_cast(bf16x8, pk);
      int r0 = wid * 32 + kt * 32 + 16 * s + 4 * hi + q4;
      const char* vp = sV + r0 * 64 + (16 * blk + 4 * p4) * 2;
      bf16x8 vf = cat8(tr_read(vp), tr_read(vp + 8 * 64));
      o = __builtin_amdgcn_mfma_f32_32x32x16_bf16(vf, pf, o, 0, 0, 0);
    }
  }
  float inv = 1.f / sum;
  u16* op = att + (long)qi * D_ + h * 32;
#pragma unroll
  for (int gq = 0; gq < 4; ++gq) {
    u32x2 w2;
    w2[0] = pack2(o[4 * gq + 0] * inv, o[4 * gq + 1] * inv);
    w2[1] = pack2(o[4 * gq + 2] * inv, o[4 * gq + 3] * inv);
    *(u32x2*)(op + 8 * gq + 4 * hi) = w2;
  }
}

using G2C = GemmCfg<64, 2, 2, 4, 4>;

__global__ __launch_bounds__(512, 2) void k_out(const u16* __restrict__ a, const u16* __restrict__ w,
                                                const u16* __restrict__ xold, u16* __restrict__ xnew,
                                                float2* __restrict__ stats) {
  __shared__ __attribute__((aligned(16))) char smem[G2C::LDS_BYTES];
  const int tid = threadIdx.x, lane = tid & 63;
  int bid = blockIdx.x;
  int xcd = bid & 7, idx = bid >> 3;
  int mblk = xcd * 4 + (idx & 3), nblk = idx >> 2;
  const int m0 = mblk * 64;
  f32x4 acc[G2C::MB][G2C::NB];
#pragma unroll
  for (int i = 0; i < G2C::MB; ++i)
#pragma unroll
    for (int j = 0; j < G2C::NB; ++j) acc[i][j] = (f32x4){0, 0, 0, 0};
  gemm_mainloop<64, 2, 2, 4, 4>([]() {}, a, D_, w, D_, m0, nblk * 128, 64, smem, acc);

  const int wid = tid >> 6, wm = wid / 4, wn = wid % 4;
  float* T = (float*)smem;
  constexpr int TP = 132;
  const int l15 = lane & 15, g = lane >> 4;
#pragma unroll
  for (int mb = 0; mb < G2C::MB; ++mb)
#pragma unroll
    for (int nb = 0; nb < G2C::NB; ++nb)
      *(f32x4*)(T + (wm * 32 + mb * 16 + l15) * TP + wn * 32 + nb * 16 + g * 4) = acc[mb][nb];
  __syncthreads();
  int row = tid >> 3, part = tid & 7;
  const u16* xo = xold + (long)(m0 + row) * D_ + nblk * 128 + part * 16;
  u16* xn = xnew + (long)(m0 + row) * D_ + nblk * 128 + part * 16;
  float v[16];
  float s = 0;
#pragma unroll
  for (int i = 0; i < 2; ++i) {
    bf16x8 xv = *(const bf16x8*)(xo + i * 8);
    u32x4 o;
#pragma unroll
    for (int j = 0; j < 4; ++j) {
      float f0 = bfr(bf2f((u16)xv[2 * j]) + T[row * TP + part * 16 + i * 8 + 2 * j]);
      float f1 = bfr(bf2f((u16)xv[2 * j + 1]) + T[row * TP + part * 16 + i * 8 + 2 * j + 1]);
      v[i * 8 + 2 * j] = f0; v[i * 8 + 2 * j + 1] = f1;
      s += f0 + f1;
      o[j] = pack2(f0, f1);
    }
    *(u32x4*)(xn + i * 8) = o;
  }
  s += __shfl_xor(s, 1); s += __shfl_xor(s, 2); s += __shfl_xor(s, 4);
  float mu = s * (1.f / 128.f), m2 = 0;
#pragma unroll
  for (int j = 0; j < 16; ++j) { float d = v[j] - mu; m2 += d * d; }
  m2 += __shfl_xor(m2, 1); m2 += __shfl_xor(m2, 2); m2 += __shfl_xor(m2, 4);
  if (part == 0) stats[(long)(m0 + row) * NPART + nblk] = make_float2(s, m2);
}

extern "C" void kernel_launch(void* const* d_in, const int* in_sizes, int n_in, void* d_out, int out_size,
                              void* d_ws, size_t ws_size, hipStream_t stream) {
  const u16* x_in = (const u16*)d_in[0];
  const u16* A_wqkv = (const u16*)d_in[1];
  const u16* A_wo = (const u16*)d_in[2];
  const u16* B_wqkv = (const u16*)d_in[4];
  const u16* B_wo = (const u16*)d_in[5];
  u16* out = (u16*)d_out;
  char* ws = (char*)d_ws;
  size_t off = 0;
  auto take = [&](size_t bytes) { char* p = ws + off; off += (bytes + 255) & ~(size_t)255; return p; };
  u16* xb0 = (u16*)take((size_t)S_ * D_ * 2);
  u16* xb1 = (u16*)take((size_t)S_ * D_ * 2);
  u16* qkv = (u16*)take((size_t)S_ * N3 * 2);
  u16* att = (u16*)take((size_t)S_ * D_ * 2);
  u16* Mm = (u16*)take((size_t)D_ * D_ * 2);
  float* kvp = (float*)take((size_t)16 * H_ * 1024 * 4);
  float* zp = (float*)take((size_t)16 * D_ * 4);
  float2* stats = (float2*)take((size_t)S_ * NPART * 8);
  float* csA = (float*)take((size_t)CSP * N3 * 4);
  float* csB = (float*)take((size_t)CSP * N3 * 4);

  k_init<<<S_ / 4 + 2 * 12 * CSP, 256, 0, stream>>>(x_in, stats, A_wqkv, B_wqkv, csA, csB);
  k_qkv<0><<<256, 512, 0, stream>>>(x_in, A_wqkv, stats, csA, att, kvp, zp);
  k_lin_mid<<<384, 256, 0, stream>>>(kvp, zp, A_wo, Mm, att);
  k_out<<<256, 512, 0, stream>>>(att, Mm, x_in, xb0, stats);
  k_qkv<1><<<256, 512, 0, stream>>>(xb0, B_wqkv, stats, csB, qkv, kvp, zp);
  k_swa<<<512, 256, 0, stream>>>(qkv, att);
  k_out<<<256, 512, 0, stream>>>(att, B_wo, xb0, xb1, stats);
  k_qkv<0><<<256, 512, 0, stream>>>(xb1, A_wqkv, stats, csA, att, kvp, zp);
  k_lin_mid<<<384, 256, 0, stream>>>(kvp, zp, A_wo, Mm, att);
  k_out<<<256, 512, 0, stream>>>(att, Mm, xb1, xb0, stats);
  k_qkv<1><<<256, 512, 0, stream>>>(xb0, B_wqkv, stats, csB, qkv, kvp, zp);
  k_swa<<<512, 256, 0, stream>>>(qkv, att);
  k_out<<<256, 512, 0, stream>>>(att, B_wo, xb0, out, stats);
}
```

```cpp
#include <hip/hip_runtime.h>
#include <hip/hip_bf16.h>
#include <stdint.h>
#include <type_traits>

constexpr int S_ = 2048, D_ = 1024, H_ = 32, DH = 32, WIN = 256;
constexpr int N3 = 3 * D_;
constexpr int NPART = 8;
constexpr int CSP = 8;

typedef __attribute__((ext_vector_type(8))) short bf16x8;
typedef __attribute__((ext_vector_type(4))) short s16x4;
typedef __attribute__((ext_vector_type(4))) float f32x4;
typedef __attribute__((ext_vector_type(16))) float f32x16;
typedef __attribute__((ext_vector_type(4))) unsigned u32x4;
typedef __attribute__((ext_vector_type(2))) unsigned u32x2;
typedef __attribute__((address_space(3))) s16x4* lds_s16x4_p;
typedef unsigned short u16;

#define DEV __device__ __forceinline__

DEV float bf2f(u16 v) { return __uint_as_float(((unsigned)v) << 16); }
DEV unsigned pack2(float lo, float hi) {
  unsigned r;
  asm("v_cvt_pk_bf16_f32 %0, %1, %2" : "=v"(r) : "v"(lo), "v"(hi));
  return r;
}
DEV u16 f2bf(float f) { return (u16)(pack2(f, f) & 0xffffu); }
DEV float bfr(float f) { return __uint_as_float(pack2(f, f) << 16); }
DEV float phi_f(float t) { return (t > 0.f ? t + 1.f : __expf(t)) + 1e-6f; }

DEV s16x4 tr_read(const char* p) {
  return __builtin_amdgcn_ds_read_tr16_b64_v4i16((lds_s16x4_p)(p));
}
DEV bf16x8 cat8(s16x4 a, s16x4 b) { return __builtin_shufflevector(a, b, 0, 1, 2, 3, 4, 5, 6, 7); }

#define WAITV(n) asm volatile("s_waitcnt vmcnt(" #n ")" ::: "memory")
template <int N> DEV void waitv() { asm volatile("s_waitcnt vmcnt(%0)" ::"n"(N) : "memory"); }
DEV void wait_lgkm0() { __builtin_amdgcn_s_waitcnt(0xC07F); }
DEV void raw_barrier() {
  asm volatile("" ::: "memory");
  wait_lgkm0();
  __builtin_amdgcn_s_barrier();
  asm volatile("" ::: "memory");
}

template <int BM, int NSEG, int WGM, int WGN, int NSTAGE>
struct GemmCfg {
  static constexpr int NW = WGM * WGN;
  static constexpr int NT_ = NW * 64;
  static constexpr int BN = NSEG * 64;
  static constexpr int WTM = BM / WGM, WTN = BN / WGN;
  static constexpr int MB = WTM / 16, NB = WTN / 16;
  static constexpr int APIECES = BM / 8, BPIECES = NSEG * 8;
  static constexpr int PPW = (APIECES + BPIECES) / NW;
  static constexpr int STAGE_BYTES = (APIECES + BPIECES) * 1024;
  static constexpr int LDS_BYTES = STAGE_BYTES * NSTAGE;
  static_assert((APIECES + BPIECES) % NW == 0, "pieces");
};

template <int BM, int NSEG, int WGM, int WGN, int NSTAGE, bool SCALE, typename PRE>
DEV void gemm_mainloop(PRE pre, const float* sinv  , const u16* __restrict__ A, int lda, const u16* __restrict__ B, int ldb,
                       int m0, int colbase0, int segstride, char* smem,
                       f32x4 (&acc)[GemmCfg<BM, NSEG, WGM, WGN, NSTAGE>::MB][GemmCfg<BM, NSEG, WGM, WGN, NSTAGE>::NB]) {
  using C = GemmCfg<BM, NSEG, WGM, WGN, NSTAGE>;
  constexpr int KT = D_ / 64;
  const int tid = threadIdx.x, lane = tid & 63;
  const int wid = __builtin_amdgcn_readfirstlane(tid >> 6);
  const int wm = wid / WGN, wn = wid % WGN;

  const u16* gp[C::PPW];
  int gstep[C::PPW];

#pragma unroll
  for (int i = 0; i < C::PPW; ++i) {
    int p = wid * C::PPW + i;
    if (p < C::APIECES) {
      int row = p * 8 + (lane >> 3), slot = lane & 7;
      int chunk = slot ^ ((row >> 1) & 7);
      gstep[i] = 64;
      gp[i] = A + (long)(m0 + row) * lda + chunk * 8;
    } else {
      int pb = p - C::APIECES;
      int seg = pb >> 3, k = (pb & 7) * 8 + (lane >> 3), slot = lane & 7;
      int f = ((k >> 1) & 1) | (((k >> 3) & 1) << 1);
      int chunk = slot ^ (f << 1);
      int cb = colbase0 + seg * segstride;
      gstep[i] = 64 * ldb;
      gp[i] = B + (long)k * ldb + cb + chunk * 8;
    }
  }
  auto issue_piece = [&](int stage, int i) {
    char* base = smem + stage * C::STAGE_BYTES + wid * C::PPW * 1024;
    __builtin_amdgcn_global_load_lds((const unsigned*)gp[i], (unsigned*)(base + i * 1024), 16, 0, 0);
    gp[i] += gstep[i];
  };

  const int l15 = lane & 15, g = lane >> 4;
  const int a_off = (wm * C::WTM + l15) * 128;
  const int a_sw = (l15 >> 1);
  const int q = (lane & 15) >> 2, p = lane & 3;
  const int fB = ((q >> 1) & 1) | ((g & 1) << 1);
  const int b_base = C::APIECES * 1024 + (8 * g + q) * 128 + (p & 1) * 8;

#pragma unroll
  for (int s = 0; s < NSTAGE; ++s)
#pragma unroll
    for (int i = 0; i < C::PPW; ++i) issue_piece(s, i);
  pre();

  bf16x8 af[2][C::MB];
  s16x4 bl[2][C::NB][2];
  constexpr int NM = C::NB * C::MB;
  constexpr int NRD = C::NB * 2 + C::MB;
  auto read1 = [&](int stage, int ks, int set, int k) {
    const char* sb = smem + stage * C::STAGE_BYTES;
    if (k < C::NB * 2) {
      int nb = k >> 1, hf = k & 1;
      int gnb = wn * C::NB + nb;
      int seg = gnb >> 2, c = gnb & 3;
      const char* bp = sb + b_base + seg * 8192 + ks * 4096 + ((((2 * c + (p >> 1)) ^ (fB << 1))) << 4);
      bl[set][nb][hf] = tr_read(bp + hf * 512);
    } else {
      int mb = k - C::NB * 2;
      af[set][mb] = *(const bf16x8*)(sb + a_off + mb * 2048 + (((ks * 4 + g) ^ a_sw) << 4));
    }
  };
  auto phase = [&](int set, bool rd, int rstage, int rks, bool iss, int dstage, int head) {
    float scl[C::MB];
    f32x4 t4[C::MB][C::NB];
    if (SCALE) {
      asm volatile("" ::: "memory");
#pragma unroll
      for (int mb = 0; mb < C::MB; ++mb) scl[mb] = sinv[(wm * C::WTM + mb * 16 + l15) * 33 + head];
    }
    constexpr int NSIDE_MAX = NRD + C::PPW;
    constexpr int PER = (NSIDE_MAX + NM - 1) / NM;
    const int nrd = rd ? NRD : 0;
    const int nside = nrd + (iss ? C::PPW : 0);
#pragma unroll
    for (int nb = 0; nb < C::NB; ++nb)
#pragma unroll
      for (int mb = 0; mb < C::MB; ++mb) {
        const int idx = nb * C::MB + mb;
        if (SCALE) {
          t4[mb][nb] = __builtin_amdgcn_mfma_f32_16x16x32_bf16(cat8(bl[set][nb][0], bl[set][nb][1]), af[set][mb], (f32x4){0, 0, 0, 0}, 0, 0, 0);
        } else {
          acc[mb][nb] = __builtin_amdgcn_mfma_f32_16x16x32_bf16(cat8(bl[set][nb][0], bl[set][nb][1]), af[set][mb], acc[mb][nb], 0, 0, 0);
        }
        __builtin_amdgcn_sched_barrier(0);
#pragma unroll
        for (int e = 0; e < PER; ++e) {
          const int k = idx * PER + e;
          if (k < nrd) read1(rstage, rks, set ^ 1, k);
          else if (k < nside) issue_piece(dstage, k - nrd);
        }
        __builtin_amdgcn_sched_barrier(0);
      }
    if (SCALE) {
#pragma unroll
      for (int nb = 0; nb < C::NB; ++nb)
#pragma unroll
        for (int mb = 0; mb < C::MB; ++mb) {
#pragma unroll
          for (int r = 0; r < 4; ++r) acc[mb][nb][r] = fmaf(t4[mb][nb][r], scl[mb], acc[mb][nb][r]);
          asm volatile("" : "+v"(acc[mb][nb]));
        }
    }
  };
  const bool grpA = wid < C::NW / 2;

  waitv<C::PPW*(NSTAGE - 1)>();
  raw_barrier();
#pragma unroll
  for (int k = 0; k < NRD; ++k) read1(0, 0, 0, k);
  auto run = [&](auto GA) {
    constexpr bool ga = decltype(GA)::value;
#pragma unroll
    for (int kt = 0; kt < KT; ++kt) {
      const int cs = kt % NSTAGE, ns = (kt + 1) % NSTAGE, ps = (kt + NSTAGE - 1) % NSTAGE;
      __builtin_amdgcn_sched_barrier(0);
      phase(0, true, cs, 1, ga && kt >= 1 && kt - 1 + NSTAGE < KT, ps, 2 * kt);
      if (kt + 1 < KT) {
        if (kt + NSTAGE - 1 < KT) waitv<C::PPW*(NSTAGE - 2)>(); else waitv<0>();
        raw_barrier();
        __builtin_amdgcn_sched_barrier(0);
        phase(1, true, ns, 0, !ga && kt + NSTAGE < KT, cs, 2 * kt + 1);
      } else {
        phase(1, false, 0, 0, false, 0, 2 * kt + 1);
      }
    }
    __builtin_amdgcn_sched_barrier(0);
  };
  if (grpA) run(std::integral_constant<bool, true>{}); else run(std::integral_constant<bool, false>{});
  raw_barrier();
}

__global__ __launch_bounds__(256) void k_init(const u16* __restrict__ x, float2* __restrict__ stats,
                                              const u16* __restrict__ wA, const u16* __restrict__ wB,
                                              float* __restrict__ csA, float* __restrict__ csB) {
  const int tid = threadIdx.x;
  if (blockIdx.x < S_ / 4) {
    int row = blockIdx.x * 4 + (tid >> 6), lane = tid & 63;
    const bf16x8* px = (const bf16x8*)(x + (long)row * D_ + lane * 16);
    bf16x8 v0 = px[0], v1 = px[1];
    float f[16];
#pragma unroll
    for (int j = 0; j < 8; ++j) { f[j] = bf2f((u16)v0[j]); f[8 + j] = bf2f((u16)v1[j]); }
    float s = 0;
#pragma unroll
    for (int j = 0; j < 16; ++j) s += f[j];
    s += __shfl_xor(s, 1); s += __shfl_xor(s, 2); s += __shfl_xor(s, 4);
    float mu = s * (1.f / 128.f), m2 = 0;
#pragma unroll
    for (int j = 0; j < 16; ++j) { float d = f[j] - mu; m2 += d * d; }
    m2 += __shfl_xor(m2, 1); m2 += __shfl_xor(m2, 2); m2 += __shfl_xor(m2, 4);
    if ((lane & 7) == 0) stats[row * NPART + (lane >> 3)] = make_float2(s, m2);
  } else {
    int b = blockIdx.x - S_ / 4;
    int wsel = b / (12 * CSP); b %= 12 * CSP;
    int cb = b / CSP, rs = b % CSP;
    const u16* w = wsel ? wB : wA;
    float* cs = wsel ? csB : csA;
    __shared__ float red[8][256];
    int cg = tid & 31, rg = tid >> 5;
    float a[8] = {0, 0, 0, 0, 0, 0, 0, 0};
    bf16x8 v[16];
#pragma unroll
    for (int i = 0; i < 16; ++i) v[i] = *(const bf16x8*)(w + (long)(rs * 128 + rg + i * 8) * N3 + cb * 256 + cg * 8);
#pragma unroll
    for (int i = 0; i < 16; ++i)
#pragma unroll
      for (int j = 0; j < 8; ++j) a[j] += bf2f((u16)v[i][j]);
#pragma unroll
    for (int j = 0; j < 8; ++j) red[rg][cg * 8 + j] = a[j];
    __syncthreads();
    float t = 0;
#pragma unroll
    for (int i = 0; i < 8; ++i) t += red[i][tid];
    cs[rs * N3 + cb * 256 + tid] = t;
  }
}

using G1C = GemmCfg<128, 3, 2, 4, 3>;
constexpr int G1_TP = 200;

template <int MODE>
__global__ __launch_bounds__(512, 2) void k_qkv(const u16* __restrict__ x, const u16* __restrict__ w,
                                                const float2* __restrict__ stats, const float* __restrict__ csp,
                                                u16* __restrict__ out  , float* __restrict__ kvp,
                                                float* __restrict__ zp) {
  __shared__ __attribute__((aligned(16))) char smem[G1C::LDS_BYTES + 2048];
  float* s_mean = (float*)(smem + G1C::LDS_BYTES);
  float* s_rstd = s_mean + 128;
  float* s_cs = s_rstd + 128;
  const int tid = threadIdx.x, lane = tid & 63;
  int bid = blockIdx.x;
  int xcd = bid & 7, idx = bid >> 3;
  int mblk = (xcd & 1) * 8 + (idx & 7), nblk = (xcd >> 1) * 4 + (idx >> 3);
  const int m0 = mblk * 128;

  f32x4 acc[G1C::MB][G1C::NB];
#pragma unroll
  for (int i = 0; i < G1C::MB; ++i)
#pragma unroll
    for (int j = 0; j < G1C::NB; ++j) acc[i][j] = (f32x4){0, 0, 0, 0};

  auto pre = [&]() {
  if (tid < 128) {
    const float2* sp = stats + (long)(m0 + tid) * NPART;
    float2 pr[NPART];
#pragma unroll
    for (int i = 0; i < NPART; ++i) pr[i] = sp[i];
    float s = 0;
#pragma unroll
    for (int i = 0; i < NPART; ++i) s += pr[i].x;
    float mean = s * (1.f / D_), m2 = 0;
#pragma unroll
    for (int i = 0; i < NPART; ++i) { float d = pr[i].x * (1.f / 128.f) - mean; m2 += pr[i].y + 128.f * d * d; }
    s_mean[tid] = mean;
    s_rstd[tid] = rsqrtf(m2 * (1.f / D_) + 1e-5f);
  } else if (tid < 128 + 192) {
    int c = tid - 128;
    int col = (c >> 6) * D_ + nblk * 64 + (c & 63);
    float t = 0;
#pragma unroll
    for (int i = 0; i < CSP; ++i) t += csp[i * N3 + col];
    s_cs[c] = t;
  }

  };
  gemm_mainloop<128, 3, 2, 4, 3, false>(pre, nullptr, x, D_, w, N3, m0, nblk * 64, D_, smem, acc);

  const int wid = tid >> 6, wm = wid / 4, wn = wid % 4;
  u16* T = (u16*)smem;
  const int l15 = lane & 15, g = lane >> 4;
#pragma unroll
  for (int mb = 0; mb < G1C::MB; ++mb) {
    const int row = wm * G1C::WTM + mb * 16 + l15;
    const float mean = s_mean[row], rstd = s_rstd[row];
#pragma unroll
    for (int nb = 0; nb < G1C::NB; ++nb) {
      const int col = wn * G1C::WTN + nb * 16 + g * 4;
      const f32x4 csv = *(const f32x4*)(s_cs + col);
      const int seg = col >> 6;
      float y[4];
#pragma unroll
      for (int r = 0; r < 4; ++r) {
        y[r] = (acc[mb][nb][r] - mean * csv[r]) * rstd;
        if (MODE == 0 && seg < 2) y[r] = phi_f(bfr(y[r]));
      }
      u32x2 w2;
      w2[0] = pack2(y[0], y[1]); w2[1] = pack2(y[2], y[3]);
      *(u32x2*)(T + row * G1_TP + col) = w2;
    }
  }
  __syncthreads();
  if (MODE == 1) {
#pragma unroll
    for (int i = 0; i < 6; ++i) {
      int ch = tid + i * 512;
      int row = ch / 24, c = ch % 24;
      u32x4 v = *(const u32x4*)(T + row * G1_TP + c * 8);
      int seg = c >> 3;
      *(u32x4*)(out + (long)(m0 + row) * N3 + seg * D_ + nblk * 64 + (c & 7) * 8) = v;
    }
  } else {
#pragma unroll
    for (int i = 0; i < 2; ++i) {
      int ch = tid + i * 512;
      int row = ch >> 3, c = ch & 7;
      u32x4 v = *(const u32x4*)(T + row * G1_TP + c * 8);
      *(u32x4*)(out + (long)(m0 + row) * D_ + nblk * 64 + c * 8) = v;
    }
    int hh = wid >> 2, dt = (wid >> 1) & 1, et = wid & 1;
    int q = (lane & 15) >> 2, p = lane & 3;
    f32x4 kv = {0, 0, 0, 0}, zz = {0, 0, 0, 0};
    bf16x8 ones;
#pragma unroll
    for (int j = 0; j < 8; ++j) ones[j] = (short)0x3F80;
#pragma unroll
    for (int ks = 0; ks < 4; ++ks) {
      int r0 = ks * 32 + 8 * g + q;
      const char* pa = (const char*)(T + r0 * G1_TP + 64 + hh * 32 + dt * 16 + 4 * p);
      const char* pb = (const char*)(T + r0 * G1_TP + 128 + hh * 32 + et * 16 + 4 * p);
      bf16x8 a = cat8(tr_read(pa), tr_read(pa + 4 * G1_TP * 2));
      bf16x8 b = cat8(tr_read(pb), tr_read(pb + 4 * G1_TP * 2));
      kv = __builtin_amdgcn_mfma_f32_16x16x32_bf16(a, b, kv, 0, 0, 0);
      zz = __builtin_amdgcn_mfma_f32_16x16x32_bf16(a, ones, zz, 0, 0, 0);
    }
    int head = nblk * 2 + hh;
    float* kp = kvp + ((long)(mblk * H_ + head) * 32) * 32;
#pragma unroll
    for (int r = 0; r < 4; ++r) {
      int d = dt * 16 + g * 4 + r;
      kp[d * 32 + et * 16 + l15] = kv[r];
      if (et == 0 && l15 == 0) zp[mblk * D_ + head * 32 + d] = zz[r];
    }
  }
}

__global__ __launch_bounds__(256) void k_lin_mid(const float* __restrict__ kvp, const float* __restrict__ zp,
                                                 const u16* __restrict__ wo, u16* __restrict__ M,
                                                 float* __restrict__ zsum) {
  constexpr int WP = 264;
  constexpr int KP = 40;
  __shared__ __attribute__((aligned(16))) u16 sw[32 * WP];
  __shared__ __attribute__((aligned(16))) u16 skv[32 * KP];
  const int tid = threadIdx.x, lane = tid & 63, wid = tid >> 6;
  const int h = blockIdx.x >> 2, nq = blockIdx.x & 3;
  f32x4 pr[16];
#pragma unroll
  for (int mb = 0; mb < 16; ++mb) pr[mb] = *(const f32x4*)(kvp + ((long)(mb * H_ + h) * 1024) + tid * 4);
  u32x4 wv[4];
#pragma unroll
  for (int i = 0; i < 4; ++i)
    wv[i] = *(const u32x4*)(wo + (long)(h * 32 + (tid >> 5) + i * 8) * D_ + nq * 256 + (tid & 31) * 8);
  if (nq == 0 && tid < 32) {
    float z = 0;
#pragma unroll
    for (int mb = 0; mb < 16; ++mb) z += zp[mb * D_ + h * 32 + tid];
    zsum[h * 32 + tid] = z;
  }
  f32x4 s4 = {0, 0, 0, 0};
#pragma unroll
  for (int mb = 0; mb < 16; ++mb) s4 += pr[mb];
  {
    u32x2 w2;
    w2[0] = pack2(s4[0], s4[1]); w2[1] = pack2(s4[2], s4[3]);
    *(u32x2*)(skv + (tid >> 3) * KP + (tid & 7) * 4) = w2;
  }
#pragma unroll
  for (int i = 0; i < 4; ++i) *(u32x4*)(sw + ((tid >> 5) + i * 8) * WP + (tid & 31) * 8) = wv[i];
  __syncthreads();
  const int l15 = lane & 15, g = lane >> 4, q = (lane & 15) >> 2, p = lane & 3;
  bf16x8 kf[2];
#pragma unroll
  for (int db = 0; db < 2; ++db) kf[db] = *(const bf16x8*)(skv + (db * 16 + l15) * KP + g * 8);
#pragma unroll
  for (int nb = 0; nb < 4; ++nb) {
    const int n0 = wid * 64 + nb * 16;
    const char* pa = (const char*)(sw + (8 * g + q) * WP + n0 + 4 * p);
    bf16x8 wf = cat8(tr_read(pa), tr_read(pa + 4 * WP * 2));
#pragma unroll
    for (int db = 0; db < 2; ++db) {
      f32x4 d4 = __builtin_amdgcn_mfma_f32_16x16x32_bf16(wf, kf[db], (f32x4){0, 0, 0, 0}, 0, 0, 0);
      u32x2 w2;
      w2[0] = pack2(d4[0], d4[1]); w2[1] = pack2(d4[2], d4[3]);
      *(u32x2*)(M + (long)(h * 32 + db * 16 + l15) * D_ + nq * 256 + n0 + 4 * g) = w2;
    }
  }
}

DEV int crow(int r, int hi) { return (r & 3) + 8 * (r >> 2) + 4 * hi; }

__global__ __launch_bounds__(256, 2) void k_swa(const u16* __restrict__ qkv, u16* __restrict__ att) {
  __shared__ __attribute__((aligned(16))) char sK[384 * 64];
  __shared__ __attribute__((aligned(16))) char sV[384 * 64];
  const int tid = threadIdx.x, lane = tid & 63, wid = tid >> 6;
  const int h = blockIdx.x & 31, qb = blockIdx.x >> 5;
  const int q0 = qb * 128, kbase = q0 - 256;
#pragma unroll
  for (int i = 0; i < 6; ++i) {
    int ch = tid + i * 256;
    int r = ch >> 2, c = ch & 3;
    int key = kbase + r;
    u32x4 kv4 = {0, 0, 0, 0}, vv4 = {0, 0, 0, 0};
    if (key >= 0) {
      kv4 = *(const u32x4*)(qkv + (long)key * N3 + D_ + h * 32 + c * 8);
      vv4 = *(const u32x4*)(qkv + (long)key * N3 + 2 * D_ + h * 32 + c * 8);
    }
    *(u32x4*)(sK + r * 64 + ((c ^ ((r >> 2) & 3)) << 4)) = kv4;
    *(u32x4*)(sV + r * 64 + (c << 4)) =vv4;
  }
  const int l31 = lane & 31, hi = lane >> 5;
  const int qw = q0 + wid * 32;
  const int qi = qw + l31;
  bf16x8 qf[2];
#pragma unroll
  for (int ks = 0; ks < 2; ++ks) qf[ks] = *(const bf16x8*)(qkv + (long)qi * N3 + h * 32 + ks * 16 + hi * 8);
  __syncthreads();

  const int kt0 = (qw >= 256) ? 0 : (256 - qw) / 32;
  f32x16 sc[9];
  const float scale_l2 = 0.17677669529663687f * 1.4426950408889634f;
  float mx = -3.0e38f;
#pragma unroll
  for (int kt = 0; kt < 9; ++kt) {
    f32x16 a16;
#pragma unroll
    for (int i = 0; i < 16; ++i) a16[i] = 0;
    if (kt >= kt0) {
      int r = wid * 32 + kt * 32 + l31;
#pragma unroll
      for (int ks = 0; ks < 2; ++ks) {
        int c = ks * 2 + hi;
        bf16x8 kf = *(const bf16x8*)(sK + r * 64 + ((c ^ ((r >> 2) & 3)) << 4));
        a16 = __builtin_amdgcn_mfma_f32_32x32x16_bf16(kf, qf[ks], a16, 0, 0, 0);
      }
      if (kt == 0) {
#pragma unroll
        for (int r2 = 0; r2 < 16; ++r2) a16[r2] = (crow(r2, hi) > l31) ? a16[r2] : -3.0e38f;
      }
      if (kt == 8) {
#pragma unroll
        for (int r2 = 0; r2 < 16; ++r2) a16[r2] = (crow(r2, hi) <= l31) ? a16[r2] : -3.0e38f;
      }
#pragma unroll
      for (int r2 = 0; r2 < 16; ++r2) mx = fmaxf(mx, a16[r2]);
    } else {
#pragma unroll
      for (int i = 0; i < 16; ++i) a16[i] = -3.0e38f;
    }
    sc[kt] = a16;
  }
  mx = fmaxf(mx, __shfl_xor(mx, 32));
  const float mxs = -mx * scale_l2;
  float sum = 0;
#pragma unroll
  for (int kt = 0; kt < 9; ++kt)
#pragma unroll
    for (int r = 0; r < 16; ++r) {
      float pv = __builtin_amdgcn_exp2f(fmaf(sc[kt][r], scale_l2, mxs));
      sc[kt][r] = pv;
      sum += pv;
    }
  sum += __shfl_xor(sum, 32);
  f32x16 o;
#pragma unroll
  for (int i = 0; i < 16; ++i) o[i] = 0;
  const int q4 = (lane & 15) >> 2, p4 = lane & 3, blk = (lane >> 4) & 1;
#pragma unroll
  for (int kt = 0; kt < 9; ++kt) {
#pragma unroll
    for (int s = 0; s < 2; ++s) {
      u32x4 pk;
#pragma unroll
      for (int j = 0; j < 4; ++j) pk[j] = pack2(sc[kt][8 * s + 2 * j], sc[kt][8 * s + 2 * j + 1]);
      bf16x8 pf = __builtin_bit_cast(bf16x8, pk);
      int r0 = wid * 32 + kt * 32 + 16 * s + 4 * hi + q4;
      const char* vp = sV + r0 * 64 + (16 * blk + 4 * p4) * 2;
      bf16x8 vf = cat8(tr_read(vp), tr_read(vp + 8 * 64));
      o = __builtin_amdgcn_mfma_f32_32x32x16_bf16(vf, pf, o, 0, 0, 0);
    }
  }
  float inv = 1.f / sum;
  u16* op = att + (long)qi * D_ + h * 32;
#pragma unroll
  for (int gq = 0; gq < 4; ++gq) {
    u32x2 w2;
    w2[0] = pack2(o[4 * gq + 0] * inv, o[4 * gq + 1] * inv);
    w2[1] = pack2(o[4 * gq + 2] * inv, o[4 * gq + 3] * inv);
    *(u32x2*)(op + 8 * gq + 4 * hi) = w2;
  }
}

using G2C = GemmCfg<64, 2, 2, 4, 4>;

template <bool LIN>
__global__ __launch_bounds__(512, 2) void k_out(const u16* __restrict__ a, const u16* __restrict__ w,
                                                const u16* __restrict__ xold, u16* __restrict__ xnew,
                                                float2* __restrict__ stats, const float* __restrict__ zsum) {
  __shared__ __attribute__((aligned(16))) char smem[G2C::LDS_BYTES + 64 * 33 * 4];
  float* sinv = (float*)(smem + G2C::LDS_BYTES);
  const int tid = threadIdx.x, lane = tid & 63;
  int bid = blockIdx.x;
  int xcd = bid & 7, idx = bid >> 3;
  int mblk = xcd * 4 + (idx & 3), nblk = idx >> 2;
  const int m0 = mblk * 64;
  f32x4 acc[G2C::MB][G2C::NB];
#pragma unroll
  for (int i = 0; i < G2C::MB; ++i)
#pragma unroll
    for (int j = 0; j < G2C::NB; ++j) acc[i][j] = (f32x4){0, 0, 0, 0};
  auto pre = [&]() {
    if (LIN) {
      const int row = tid >> 3, hq = tid & 7;
      const u16* pp = a + (long)(m0 + row) * D_ + hq * 128;
#pragma unroll
      for (int hh = 0; hh < 4; ++hh) {
        float den = 0;
#pragma unroll
        for (int i = 0; i < 4; ++i) {
          bf16x8 v = *(const bf16x8*)(pp + hh * 32 + i * 8);
          f32x4 z0 = *(const f32x4*)(zsum + hq * 128 + hh * 32 + i * 8), z1 = *(const f32x4*)(zsum + hq * 128 + hh * 32 + i * 8 + 4);
#pragma unroll
          for (int j = 0; j < 4; ++j) den += bf2f((u16)v[j]) * z0[j] + bf2f((u16)v[4 + j]) * z1[j];
        }
        sinv[row * 33 + hq * 4 + hh] = 1.f / (den + 1e-6f);
      }
    }
  };
  gemm_mainloop<64, 2, 2, 4, 4, LIN>(pre, sinv, a, D_, w, D_, m0, nblk * 128, 64, smem, acc);

  const int wid = tid >> 6, wm = wid / 4, wn = wid % 4;
  float* T = (float*)smem;
  constexpr int TP = 132;
  const int l15 = lane & 15, g = lane >> 4;
#pragma unroll
  for (int mb = 0; mb < G2C::MB; ++mb)
#pragma unroll
    for (int nb = 0; nb < G2C::NB; ++nb)
      *(f32x4*)(T + (wm * 32 + mb * 16 + l15) * TP + wn * 32 + nb * 16 + g * 4) = acc[mb][nb];
  __syncthreads();
  int row = tid >> 3, part = tid & 7;
  const u16* xo = xold + (long)(m0 + row) * D_ + nblk * 128 + part * 16;
  u16* xn = xnew + (long)(m0 + row) * D_ + nblk * 128 + part * 16;
  float v[16];
  float s = 0;
#pragma unroll
  for (int i = 0; i < 2; ++i) {
    bf16x8 xv = *(const bf16x8*)(xo + i * 8);
    u32x4 o;
#pragma unroll
    for (int j = 0; j < 4; ++j) {
      float f0 = bfr(bf2f((u16)xv[2 * j]) + T[row * TP + part * 16 + i * 8 + 2 * j]);
      float f1 = bfr(bf2f((u16)xv[2 * j + 1]) + T[row * TP + part * 16 + i * 8 + 2 * j + 1]);
      v[i * 8 + 2 * j] = f0; v[i * 8 + 2 * j + 1] = f1;
      s += f0 + f1;
      o[j] = pack2(f0, f1);
    }
    *(u32x4*)(xn + i * 8) = o;
  }
  s += __shfl_xor(s, 1); s += __shfl_xor(s, 2); s += __shfl_xor(s, 4);
  float mu = s * (1.f / 128.f), m2 = 0;
#pragma unroll
  for (int j = 0; j < 16; ++j) { float d = v[j] - mu; m2 += d * d; }
  m2 += __shfl_xor(m2, 1); m2 += __shfl_xor(m2, 2); m2 += __shfl_xor(m2, 4);
  if (part == 0) stats[(long)(m0 + row) * NPART + nblk] = make_float2(s, m2);
}

extern "C" void kernel_launch(void* const* d_in, const int* in_sizes, int n_in, void* d_out, int out_size,
                              void* d_ws, size_t ws_size, hipStream_t stream) {
  const u16* x_in = (const u16*)d_in[0];
  const u16* A_wqkv = (const u16*)d_in[1];
  const u16* A_wo = (const u16*)d_in[2];
  const u16* B_wqkv = (const u16*)d_in[4];
  const u16* B_wo = (const u16*)d_in[5];
  u16* out = (u16*)d_out;
  char* ws = (char*)d_ws;
  size_t off = 0;
  auto take = [&](size_t bytes) { char* p = ws + off; off += (bytes + 255) & ~(size_t)255; return p; };
  u16* xb0 = (u16*)take((size_t)S_ * D_ * 2);
  u16* xb1 = (u16*)take((size_t)S_ * D_ * 2);
  u16* qkv = (u16*)take((size_t)S_ * N3 * 2);
  u16* att = (u16*)take((size_t)S_ * D_ * 2);
  u16* Mm = (u16*)take((size_t)D_ * D_ * 2);
  float* kvp = (float*)take((size_t)16 * H_ * 1024 * 4);
  float* zp = (float*)take((size_t)16 * D_ * 4);
  float2* stats = (float2*)take((size_t)S_ * NPART * 8);
  float* csA = (float*)take((size_t)CSP * N3 * 4);
  float* csB = (float*)take((size_t)CSP * N3 * 4);
  float* zsum = (float*)take((size_t)D_ * 4);

  k_init<<<S_ / 4 + 2 * 12 * CSP, 256, 0, stream>>>(x_in, stats, A_wqkv, B_wqkv, csA, csB);
  k_qkv<0><<<256, 512, 0, stream>>>(x_in, A_wqkv, stats, csA, att, kvp, zp);
  k_lin_mid<<<128, 256, 0, stream>>>(kvp, zp, A_wo, Mm, zsum);
  k_out<true><<<256, 512, 0, stream>>>(att, Mm, x_in, xb0, stats, zsum);
  k_qkv<1><<<256, 512, 0, stream>>>(xb0, B_wqkv, stats, csB, qkv, kvp, zp);
  k_swa<<<512, 256, 0, stream>>>(qkv, att);
  k_out<false><<<256, 512, 0, stream>>>(att, B_wo, xb0, xb1, stats, zsum);
  k_qkv<0><<<256, 512, 0, stream>>>(xb1, A_wqkv, stats, csA, att, kvp, zp);
  k_lin_mid<<<128, 256, 0, stream>>>(kvp, zp, A_wo, Mm, zsum);
  k_out<true><<<256, 512, 0, stream>>>(att, Mm, xb1, xb0, stats, zsum);
  k_qkv<1><<<256, 512, 0, stream>>>(xb0, B_wqkv, stats, csB, qkv, kvp, zp);
  k_swa<<<512, 256, 0, stream>>>(qkv, att);
  k_out<false><<<256, 512, 0, stream>>>(att, B_wo, xb0, out, stats, zsum);
}
```

```cpp
#include <hip/hip_runtime.h>
#include <hip/hip_bf16.h>
#include <stdint.h>
#include <type_traits>

constexpr int S_ = 2048, D_ = 1024, H_ = 32, DH = 32, WIN = 256;
constexpr int N3 = 3 * D_;
constexpr int NPART = 8;
constexpr int CSP = 8;

typedef __attribute__((ext_vector_type(8))) short bf16x8;
typedef __attribute__((ext_vector_type(4))) short s16x4;
typedef __attribute__((ext_vector_type(4))) float f32x4;
typedef __attribute__((ext_vector_type(16))) float f32x16;
typedef __attribute__((ext_vector_type(4))) unsigned u32x4;
typedef __attribute__((ext_vector_type(2))) unsigned u32x2;
typedef __attribute__((address_space(3))) s16x4* lds_s16x4_p;
typedef unsigned short u16;

#define DEV __device__ __forceinline__

DEV float bf2f(u16 v) { return __uint_as_float(((unsigned)v) << 16); }
DEV unsigned pack2(float lo, float hi) {
  unsigned r;
  asm("v_cvt_pk_bf16_f32 %0, %1, %2" : "=v"(r) : "v"(lo), "v"(hi));
  return r;
}
DEV u16 f2bf(float f) { return (u16)(pack2(f, f) & 0xffffu); }
DEV float bfr(float f) { return __uint_as_float(pack2(f, f) << 16); }
DEV float phi_f(float t) { return (t > 0.f ? t + 1.f : __expf(t)) + 1e-6f; }

DEV s16x4 tr_read(const char* p) {
  return __builtin_amdgcn_ds_read_tr16_b64_v4i16((lds_s16x4_p)(p));
}
DEV bf16x8 cat8(s16x4 a, s16x4 b) { return __builtin_shufflevector(a, b, 0, 1, 2, 3, 4, 5, 6, 7); }

#define WAITV(n) asm volatile("s_waitcnt vmcnt(" #n ")" ::: "memory")
template <int N> DEV void waitv() { asm volatile("s_waitcnt vmcnt(%0)" ::"n"(N) : "memory"); }
DEV void wait_lgkm0() { __builtin_amdgcn_s_waitcnt(0xC07F); }
DEV void raw_barrier() {
  asm volatile("" ::: "memory");
  wait_lgkm0();
  __builtin_amdgcn_s_barrier();
  asm volatile("" ::: "memory");
}

template <int BM, int NSEG, int WGM, int WGN, int NSTAGE>
struct GemmCfg {
  static constexpr int NW = WGM * WGN;
  static constexpr int NT_ = NW * 64;
  static constexpr int BN = NSEG * 64;
  static constexpr int WTM = BM / WGM, WTN = BN / WGN;
  static constexpr int MB = WTM / 16, NB = WTN / 16;
  static constexpr int APIECES = BM / 8, BPIECES = NSEG * 8;
  static constexpr int PPW = (APIECES + BPIECES) / NW;
  static constexpr int STAGE_BYTES = (APIECES + BPIECES) * 1024;
  static constexpr int LDS_BYTES = STAGE_BYTES * NSTAGE;
  static_assert((APIECES + BPIECES) % NW == 0, "pieces");
};

template <int BM, int NSEG, int WGM, int WGN, int NSTAGE, bool SCALE, typename PRE>
DEV void gemm_mainloop(PRE pre, const float* sinv  , const u16* __restrict__ A, int lda, const u16* __restrict__ B, int ldb,
                       int m0, int colbase0, int segstride, char* smem,
                       f32x4 (&acc)[GemmCfg<BM, NSEG, WGM, WGN, NSTAGE>::MB][GemmCfg<BM, NSEG, WGM, WGN, NSTAGE>::NB]) {
  using C = GemmCfg<BM, NSEG, WGM, WGN, NSTAGE>;
  constexpr int KT = D_ / 64;
  const int tid = threadIdx.x, lane = tid & 63;
  const int wid = __builtin_amdgcn_readfirstlane(tid >> 6);
  const int wm = wid / WGN, wn = wid % WGN;

  const u16* gp[C::PPW];
  int gstep[C::PPW];

#pragma unroll
  for (int i = 0; i < C::PPW; ++i) {
    int p = wid * C::PPW + i;
    if (p < C::APIECES) {
      int row = p * 8 + (lane >> 3), slot = lane & 7;
      int chunk = slot ^ ((row >> 1) & 7);
      gstep[i] = 64;
      gp[i] = A + (long)(m0 + row) * lda + chunk * 8;
    } else {
      int pb = p - C::APIECES;
      int seg = pb >> 3, k = (pb & 7) * 8 + (lane >> 3), slot = lane & 7;
      int f = ((k >> 1) & 1) | (((k >> 3) & 1) << 1);
      int chunk = slot ^ (f << 1);
      int cb = colbase0 + seg * segstride;
      gstep[i] = 64 * ldb;
      gp[i] = B + (long)k * ldb + cb + chunk * 8;
    }
  }
  auto issue_piece = [&](int stage, int i) {
    char* base = smem + stage * C::STAGE_BYTES + wid * C::PPW * 1024;
    __builtin_amdgcn_global_load_lds((const unsigned*)gp[i], (unsigned*)(base + i * 1024), 16, 0, 0);
    gp[i] += gstep[i];
  };

  const int l15 = lane & 15, g = lane >> 4;
  const int a_off = (wm * C::WTM + l15) * 128;
  const int a_sw = (l15 >> 1);
  const int q = (lane & 15) >> 2, p = lane & 3;
  const int fB = ((q >> 1) & 1) | ((g & 1) << 1);
  const int b_base = C::APIECES * 1024 + (8 * g + q) * 128 + (p & 1) * 8;

#pragma unroll
  for (int s = 0; s < NSTAGE; ++s)
#pragma unroll
    for (int i = 0; i < C::PPW; ++i) issue_piece(s, i);
  pre();

  bf16x8 af[2][C::MB];
  s16x4 bl[2][C::NB][2];
  constexpr int NM = C::NB * C::MB;
  constexpr int NRD = C::NB * 2 + C::MB;
  auto read1 = [&](int stage, int ks, int set, int k) {
    const char* sb = smem + stage * C::STAGE_BYTES;
    if (k < C::NB * 2) {
      int nb = k >> 1, hf = k & 1;
      int gnb = wn * C::NB + nb;
      int seg = gnb >> 2, c = gnb & 3;
      const char* bp = sb + b_base + seg * 8192 + ks * 4096 + ((((2 * c + (p >> 1)) ^ (fB << 1))) << 4);
      bl[set][nb][hf] = tr_read(bp + hf * 512);
    } else {
      int mb = k - C::NB * 2;
      af[set][mb] = *(const bf16x8*)(sb + a_off + mb * 2048 + (((ks * 4 + g) ^ a_sw) << 4));
    }
  };
  auto phase = [&](int set, bool rd, int rstage, int rks, bool iss, int dstage, int head) {
    float scl[C::MB];
    f32x4 t4[C::MB][C::NB];
    if (SCALE) {
      asm volatile("" ::: "memory");
#pragma unroll
      for (int mb = 0; mb < C::MB; ++mb) scl[mb] = sinv[(wm * C::WTM + mb * 16 + l15) * 33 + head];
    }
    constexpr int NSIDE_MAX = NRD + C::PPW;
    constexpr int PER = (NSIDE_MAX + NM - 1) / NM;
    const int nrd = rd ? NRD : 0;
    const int nside = nrd + (iss ? C::PPW : 0);
#pragma unroll
    for (int nb = 0; nb < C::NB; ++nb)
#pragma unroll
      for (int mb = 0; mb < C::MB; ++mb) {
        const int idx = nb * C::MB + mb;
        if (SCALE) {
          t4[mb][nb] = __builtin_amdgcn_mfma_f32_16x16x32_bf16(cat8(bl[set][nb][0], bl[set][nb][1]), af[set][mb], (f32x4){0, 0, 0, 0}, 0, 0, 0);
        } else {
          acc[mb][nb] = __builtin_amdgcn_mfma_f32_16x16x32_bf16(cat8(bl[set][nb][0], bl[set][nb][1]), af[set][mb], acc[mb][nb], 0, 0, 0);
        }
        __builtin_amdgcn_sched_barrier(0);
#pragma unroll
        for (int e = 0; e < PER; ++e) {
          const int k = idx * PER + e;
          if (k < nrd) read1(rstage, rks, set ^ 1, k);
          else if (k < nside) issue_piece(dstage, k - nrd);
        }
        __builtin_amdgcn_sched_barrier(0);
      }
    if (SCALE) {
#pragma unroll
      for (int nb = 0; nb < C::NB; ++nb)
#pragma unroll
        for (int mb = 0; mb < C::MB; ++mb) {
#pragma unroll
          for (int r = 0; r < 4; ++r) acc[mb][nb][r] = fmaf(t4[mb][nb][r], scl[mb], acc[mb][nb][r]);
          asm volatile("" : "+v"(acc[mb][nb]));
        }
    }
  };
  const bool grpA = wid < C::NW / 2;

  waitv<C::PPW*(NSTAGE - 1)>();
  raw_barrier();
#pragma unroll
  for (int k = 0; k < NRD; ++k) read1(0, 0, 0, k);
  auto run = [&](auto GA) {
    constexpr bool ga = decltype(GA)::value;
#pragma unroll
    for (int kt = 0; kt < KT; ++kt) {
      const int cs = kt % NSTAGE, ns = (kt + 1) % NSTAGE, ps = (kt + NSTAGE - 1) % NSTAGE;
      __builtin_amdgcn_sched_barrier(0);
      phase(0, true, cs, 1, ga && kt >= 1 && kt - 1 + NSTAGE < KT, ps, 2 * kt);
      if (kt + 1 < KT) {
        if (kt + NSTAGE - 1 < KT) waitv<C::PPW*(NSTAGE - 2)>(); else waitv<0>();
        raw_barrier();
        __builtin_amdgcn_sched_barrier(0);
        phase(1, true, ns, 0, !ga && kt + NSTAGE < KT, cs, 2 * kt + 1);
      } else {
        phase(1, false, 0, 0, false, 0, 2 * kt + 1);
      }
    }
    __builtin_amdgcn_sched_barrier(0);
  };
  if (grpA) run(std::integral_constant<bool, true>{}); else run(std::integral_constant<bool, false>{});
  raw_barrier();
}

__global__ __launch_bounds__(256) void k_init(const u16* __restrict__ x, float2* __restrict__ stats,
                                              const u16* __restrict__ wA, const u16* __restrict__ wB,
                                              float* __restrict__ csA, float* __restrict__ csB) {
  const int tid = threadIdx.x;
  if (blockIdx.x < S_ / 4) {
    int row = blockIdx.x * 4 + (tid >> 6), lane = tid & 63;
    const bf16x8* px = (const bf16x8*)(x + (long)row * D_ + lane * 16);
    bf16x8 v0 = px[0], v1 = px[1];
    float f[16];
#pragma unroll
    for (int j = 0; j < 8; ++j) { f[j] = bf2f((u16)v0[j]); f[8 + j] = bf2f((u16)v1[j]); }
    float s = 0;
#pragma unroll
    for (int j = 0; j < 16; ++j) s += f[j];
    s += __shfl_xor(s, 1); s += __shfl_xor(s, 2); s += __shfl_xor(s, 4);
    float mu = s * (1.f / 128.f), m2 = 0;
#pragma unroll
    for (int j = 0; j < 16; ++j) { float d = f[j] - mu; m2 += d * d; }
    m2 += __shfl_xor(m2, 1); m2 += __shfl_xor(m2, 2); m2 += __shfl_xor(m2, 4);
    if ((lane & 7) == 0) stats[row * NPART + (lane >> 3)] = make_float2(s, m2);
  } else {
    int b = blockIdx.x - S_ / 4;
    int wsel = b / (12 * CSP); b %= 12 * CSP;
    int cb = b / CSP, rs = b % CSP;
    const u16* w = wsel ? wB : wA;
    float* cs = wsel ? csB : csA;
    __shared__ float red[8][256];
    int cg = tid & 31, rg = tid >> 5;
    float a[8] = {0, 0, 0, 0, 0, 0, 0, 0};
    bf16x8 v[16];
#pragma unroll
    for (int i = 0; i < 16; ++i) v[i] = *(const bf16x8*)(w + (long)(rs * 128 + rg + i * 8) * N3 + cb * 256 + cg * 8);
#pragma unroll
    for (int i = 0; i < 16; ++i)
#pragma unroll
      for (int j = 0; j < 8; ++j) a[j] += bf2f((u16)v[i][j]);
#pragma unroll
    for (int j = 0; j < 8; ++j) red[rg][cg * 8 + j] = a[j];
    __syncthreads();
    float t = 0;
#pragma unroll
    for (int i = 0; i < 8; ++i) t += red[i][tid];
    cs[rs * N3 + cb * 256 + tid] = t;
  }
}

using G1C = GemmCfg<128, 3, 2, 4, 3>;
constexpr int G1_TP = 200;

template <int MODE>
__global__ __launch_bounds__(512, 2) void k_qkv(const u16* __restrict__ x, const u16* __restrict__ w,
                                                const float2* __restrict__ stats, const float* __restrict__ csp,
                                                u16* __restrict__ out  , float* __restrict__ kvp,
                                                float* __restrict__ zp) {
  __shared__ __attribute__((aligned(16))) char smem[G1C::LDS_BYTES + 2048];
  float* s_mean = (float*)(smem + G1C::LDS_BYTES);
  float* s_rstd = s_mean + 128;
  float* s_cs = s_rstd + 128;
  const int tid = threadIdx.x, lane = tid & 63;
  int bid = blockIdx.x;
  int xcd = bid & 7, idx = bid >> 3;
  int mblk = (xcd & 1) * 8 + (idx & 7), nblk = (xcd >> 1) * 4 + (idx >> 3);
  const int m0 = mblk * 128;

  f32x4 acc[G1C::MB][G1C::NB];
#pragma unroll
  for (int i = 0; i < G1C::MB; ++i)
#pragma unroll
    for (int j = 0; j < G1C::NB; ++j) acc[i][j] = (f32x4){0, 0, 0, 0};

  auto pre = [&]() {
  if (tid < 128) {
    const float2* sp = stats + (long)(m0 + tid) * NPART;
    float2 pr[NPART];
#pragma unroll
    for (int i = 0; i < NPART; ++i) pr[i] = sp[i];
    float s = 0;
#pragma unroll
    for (int i = 0; i < NPART; ++i) s += pr[i].x;
    float mean = s * (1.f / D_), m2 = 0;
#pragma unroll
    for (int i = 0; i < NPART; ++i) { float d = pr[i].x * (1.f / 128.f) - mean; m2 += pr[i].y + 128.f * d * d; }
    s_mean[tid] = mean;
    s_rstd[tid] = rsqrtf(m2 * (1.f / D_) + 1e-5f);
  } else if (tid < 128 + 192) {
    int c = tid - 128;
    int col = (c >> 6) * D_ + nblk * 64 + (c & 63);
    float t = 0;
#pragma unroll
    for (int i = 0; i < CSP; ++i) t += csp[i * N3 + col];
    s_cs[c] = t;
  }

  };
  gemm_mainloop<128, 3, 2, 4, 3, false>(pre, nullptr, x, D_, w, N3, m0, nblk * 64, D_, smem, acc);

  const int wid = tid >> 6, wm = wid / 4, wn = wid % 4;
  u16* T = (u16*)smem;
  const int l15 = lane & 15, g = lane >> 4;
#pragma unroll
  for (int mb = 0; mb < G1C::MB; ++mb) {
    const int row = wm * G1C::WTM + mb * 16 + l15;
    const float mean = s_mean[row], rstd = s_rstd[row];
#pragma unroll
    for (int nb = 0; nb < G1C::NB; ++nb) {
      const int col = wn * G1C::WTN + nb * 16 + g * 4;
      const f32x4 csv = *(const f32x4*)(s_cs + col);
      const int seg = col >> 6;
      float y[4];
#pragma unroll
      for (int r = 0; r < 4; ++r) {
        y[r] = (acc[mb][nb][r] - mean * csv[r]) * rstd;
        if (MODE == 0 && seg < 2) y[r] = phi_f(bfr(y[r]));
      }
      u32x2 w2;
      w2[0] = pack2(y[0], y[1]); w2[1] = pack2(y[2], y[3]);
      *(u32x2*)(T + row * G1_TP + col) = w2;
    }
  }
  __syncthreads();
  if (MODE == 1) {
#pragma unroll
    for (int i = 0; i < 6; ++i) {
      int ch = tid + i * 512;
      int row = ch / 24, c = ch % 24;
      u32x4 v = *(const u32x4*)(T + row * G1_TP + c * 8);
      int seg = c >> 3;
      *(u32x4*)(out + (long)(m0 + row) * N3 + seg * D_ + nblk * 64 + (c & 7) * 8) = v;
    }
  } else {
#pragma unroll
    for (int i = 0; i < 2; ++i) {
      int ch = tid + i * 512;
      int row = ch >> 3, c = ch & 7;
      u32x4 v = *(const u32x4*)(T + row * G1_TP + c * 8);
      *(u32x4*)(out + (long)(m0 + row) * D_ + nblk * 64 + c * 8) = v;
    }
    int hh = wid >> 2, dt = (wid >> 1) & 1, et = wid & 1;
    int q = (lane & 15) >> 2, p = lane & 3;
    f32x4 kv = {0, 0, 0, 0}, zz = {0, 0, 0, 0};
    bf16x8 ones;
#pragma unroll
    for (int j = 0; j < 8; ++j) ones[j] = (short)0x3F80;
#pragma unroll
    for (int ks = 0; ks < 4; ++ks) {
      int r0 = ks * 32 + 8 * g + q;
      const char* pa = (const char*)(T + r0 * G1_TP + 64 + hh * 32 + dt * 16 + 4 * p);
      const char* pb = (const char*)(T + r0 * G1_TP + 128 + hh * 32 + et * 16 + 4 * p);
      bf16x8 a = cat8(tr_read(pa), tr_read(pa + 4 * G1_TP * 2));
      bf16x8 b = cat8(tr_read(pb), tr_read(pb + 4 * G1_TP * 2));
      kv = __builtin_amdgcn_mfma_f32_16x16x32_bf16(a, b, kv, 0, 0, 0);
      zz = __builtin_amdgcn_mfma_f32_16x16x32_bf16(a, ones, zz, 0, 0, 0);
    }
    int head = nblk * 2 + hh;
    float* kp = kvp + ((long)(mblk * H_ + head) * 32) * 32;
#pragma unroll
    for (int r = 0; r < 4; ++r) {
      int d = dt * 16 + g * 4 + r;
      kp[d * 32 + et * 16 + l15] = kv[r];
      if (et == 0 && l15 == 0) zp[mblk * D_ + head * 32 + d] = zz[r];
    }
  }
}

__global__ __launch_bounds__(256) void k_lin_mid(const float* __restrict__ kvp, const float* __restrict__ zp,
                                                 const u16* __restrict__ wo, u16* __restrict__ M,
                                                 const u16* __restrict__ pq, float* __restrict__ inv) {
  constexpr int WP = 264;
  constexpr int KP = 40;
  __shared__ __attribute__((aligned(16))) u16 sw[32 * WP];
  __shared__ __attribute__((aligned(16))) u16 skv[32 * KP];
  const int tid = threadIdx.x, lane = tid & 63, wid = tid >> 6;
  if (blockIdx.x >= 128) {
    const int r0 = (blockIdx.x - 128) * 8 + wid * 2;
    float* zs = (float*)sw;
    f32x4 zpr[16];
#pragma unroll
    for (int mb = 0; mb < 16; ++mb) zpr[mb] = *(const f32x4*)(zp + mb * D_ + tid * 4);
    bf16x8 v[2][2];
#pragma unroll
    for (int r = 0; r < 2; ++r) {
      const u16* pp = pq + (long)(r0 + r) * D_ + lane * 16;
      v[r][0] = *(const bf16x8*)(pp);
      v[r][1] = *(const bf16x8*)(pp + 8);
    }
    f32x4 zs4 = {0, 0, 0, 0};
#pragma unroll
    for (int mb = 0; mb < 16; ++mb) zs4 += zpr[mb];
    *(f32x4*)(zs + tid * 4) = zs4;
    __syncthreads();
    f32x4 z4[4];
#pragma unroll
    for (int i = 0; i < 4; ++i) z4[i] = *(const f32x4*)(zs + lane * 16 + i * 4);
#pragma unroll
    for (int r = 0; r < 2; ++r) {
      float den = 0;
#pragma unroll
      for (int i = 0; i < 2; ++i)
#pragma unroll
        for (int j = 0; j < 4; ++j)
          den += bf2f((u16)v[r][i][j]) * z4[2 * i][j] + bf2f((u16)v[r][i][4 + j]) * z4[2 * i + 1][j];
      den += __shfl_xor(den, 1);
      if ((lane & 1) == 0) inv[(r0 + r) * 32 + (lane >> 1)] = 1.f / (den + 1e-6f);
    }
    return;
  }
  const int h = blockIdx.x >> 2, nq = blockIdx.x & 3;
  f32x4 pr[16];
#pragma unroll
  for (int mb = 0; mb < 16; ++mb) pr[mb] = *(const f32x4*)(kvp + ((long)(mb * H_ + h) * 1024) + tid * 4);
  u32x4 wv[4];
#pragma unroll
  for (int i = 0; i < 4; ++i)
    wv[i] = *(const u32x4*)(wo + (long)(h * 32 + (tid >> 5) + i * 8) * D_ + nq * 256 + (tid & 31) * 8);
  f32x4 s4 = {0, 0, 0, 0};
#pragma unroll
  for (int mb = 0; mb < 16; ++mb) s4 += pr[mb];
  {
    u32x2 w2;
    w2[0] = pack2(s4[0], s4[1]); w2[1] = pack2(s4[2], s4[3]);
    *(u32x2*)(skv + (tid >> 3) * KP + (tid & 7) * 4) = w2;
  }
#pragma unroll
  for (int i = 0; i < 4; ++i) *(u32x4*)(sw + ((tid >> 5) + i * 8) * WP + (tid & 31) * 8) = wv[i];
  __syncthreads();
  const int l15 = lane & 15, g = lane >> 4, q = (lane & 15) >> 2, p = lane & 3;
  bf16x8 kf[2];
#pragma unroll
  for (int db = 0; db < 2; ++db) kf[db] = *(const bf16x8*)(skv + (db * 16 + l15) * KP + g * 8);
#pragma unroll
  for (int nb = 0; nb < 4; ++nb) {
    const int n0 = wid * 64 + nb * 16;
    const char* pa = (const char*)(sw + (8 * g + q) * WP + n0 + 4 * p);
    bf16x8 wf = cat8(tr_read(pa), tr_read(pa + 4 * WP * 2));
#pragma unroll
    for (int db = 0; db < 2; ++db) {
      f32x4 d4 = __builtin_amdgcn_mfma_f32_16x16x32_bf16(wf, kf[db], (f32x4){0, 0, 0, 0}, 0, 0, 0);
      u32x2 w2;
      w2[0] = pack2(d4[0], d4[1]); w2[1] = pack2(d4[2], d4[3]);
      *(u32x2*)(M + (long)(h * 32 + db * 16 + l15) * D_ + nq * 256 + n0 + 4 * g) = w2;
    }
  }
}

DEV int crow(int r, int hi) { return (r & 3) + 8 * (r >> 2) + 4 * hi; }

__global__ __launch_bounds__(256, 2) void k_swa(const u16* __restrict__ qkv, u16* __restrict__ att) {
  __shared__ __attribute__((aligned(16))) char sK[384 * 64];
  __shared__ __attribute__((aligned(16))) char sV[384 * 64];
  const int tid = threadIdx.x, lane = tid & 63, wid = tid >> 6;
  const int h = blockIdx.x & 31, qb = blockIdx.x >> 5;
  const int q0 = qb * 128, kbase = q0 - 256;
#pragma unroll
  for (int i = 0; i < 6; ++i) {
    int ch = tid + i * 256;
    int r = ch >> 2, c = ch & 3;
    int key = kbase + r;
    u32x4 kv4 = {0, 0, 0, 0}, vv4 = {0, 0, 0, 0};
    if (key >= 0) {
      kv4 = *(const u32x4*)(qkv + (long)key * N3 + D_ + h * 32 + c * 8);
      vv4 = *(const u32x4*)(qkv + (long)key * N3 + 2 * D_ + h * 32 + c * 8);
    }
    *(u32x4*)(sK + r * 64 + ((c ^ ((r >> 2) & 3)) << 4)) = kv4;
    *(u32x4*)(sV + r * 64 + (c << 4)) =vv4;
  }
  const int l31 = lane & 31, hi = lane >> 5;
  const int qw = q0 + wid * 32;
  const int qi = qw + l31;
  bf16x8 qf[2];
#pragma unroll
  for (int ks = 0; ks < 2; ++ks) qf[ks] = *(const bf16x8*)(qkv + (long)qi * N3 + h * 32 + ks * 16 + hi * 8);
  __syncthreads();

  const int kt0 = (qw >= 256) ? 0 : (256 - qw) / 32;
  f32x16 sc[9];
  const float scale_l2 = 0.17677669529663687f * 1.4426950408889634f;
  float mx = -3.0e38f;
#pragma unroll
  for (int kt = 0; kt < 9; ++kt) {
    f32x16 a16;
#pragma unroll
    for (int i = 0; i < 16; ++i) a16[i] = 0;
    if (kt >= kt0) {
      int r = wid * 32 + kt * 32 + l31;
#pragma unroll
      for (int ks = 0; ks < 2; ++ks) {
        int c = ks * 2 + hi;
        bf16x8 kf = *(const bf16x8*)(sK + r * 64 + ((c ^ ((r >> 2) & 3)) << 4));
        a16 = __builtin_amdgcn_mfma_f32_32x32x16_bf16(kf, qf[ks], a16, 0, 0, 0);
      }
      if (kt == 0) {
#pragma unroll
        for (int r2 = 0; r2 < 16; ++r2) a16[r2] = (crow(r2, hi) > l31) ? a16[r2] : -3.0e38f;
      }
      if (kt == 8) {
#pragma unroll
        for (int r2 = 0; r2 < 16; ++r2) a16[r2] = (crow(r2, hi) <= l31) ? a16[r2] : -3.0e38f;
      }
#pragma unroll
      for (int r2 = 0; r2 < 16; ++r2) mx = fmaxf(mx, a16[r2]);
    } else {
#pragma unroll
      for (int i = 0; i < 16; ++i) a16[i] = -3.0e38f;
    }
    sc[kt] = a16;
  }
  mx = fmaxf(mx, __shfl_xor(mx, 32));
  const float mxs = -mx * scale_l2;
  float sum = 0;
#pragma unroll
  for (int kt = 0; kt < 9; ++kt)
#pragma unroll
    for (int r = 0; r < 16; ++r) {
      float pv = __builtin_amdgcn_exp2f(fmaf(sc[kt][r], scale_l2, mxs));
      sc[kt][r] = pv;
      sum += pv;
    }
  sum += __shfl_xor(sum, 32);
  f32x16 o;
#pragma unroll
  for (int i = 0; i < 16; ++i) o[i] = 0;
  const int q4 = (lane & 15) >> 2, p4 = lane & 3, blk = (lane >> 4) & 1;
#pragma unroll
  for (int kt = 0; kt < 9; ++kt) {
#pragma unroll
    for (int s = 0; s < 2; ++s) {
      u32x4 pk;
#pragma unroll
      for (int j = 0; j < 4; ++j) pk[j] = pack2(sc[kt][8 * s + 2 * j], sc[kt][8 * s + 2 * j + 1]);
      bf16x8 pf = __builtin_bit_cast(bf16x8, pk);
      int r0 = wid * 32 + kt * 32 + 16 * s + 4 * hi + q4;
      const char* vp = sV + r0 * 64 + (16 * blk + 4 * p4) * 2;
      bf16x8 vf = cat8(tr_read(vp), tr_read(vp + 8 * 64));
      o = __builtin_amdgcn_mfma_f32_32x32x16_bf16(vf, pf, o, 0, 0, 0);
    }
  }
  float inv = 1.f / sum;
  u16* op = att + (long)qi * D_ + h * 32;
#pragma unroll
  for (int gq = 0; gq < 4; ++gq) {
    u32x2 w2;
    w2[0] = pack2(o[4 * gq + 0] * inv, o[4 * gq + 1] * inv);
    w2[1] = pack2(o[4 * gq + 2] * inv, o[4 * gq + 3] * inv);
    *(u32x2*)(op + 8 * gq + 4 * hi) = w2;
  }
}

using G2C = GemmCfg<64, 2, 2, 4, 4>;

template <bool LIN>
__global__ __launch_bounds__(512, 2) void k_out(const u16* __restrict__ a, const u16* __restrict__ w,
                                                const u16* __restrict__ xold, u16* __restrict__ xnew,
                                                float2* __restrict__ stats, const float* __restrict__ zsum) {
  __shared__ __attribute__((aligned(16))) char smem[G2C::LDS_BYTES + 64 * 33 * 4];
  float* sinv = (float*)(smem + G2C::LDS_BYTES);
  const int tid = threadIdx.x, lane = tid & 63;
  int bid = blockIdx.x;
  int xcd = bid & 7, idx = bid >> 3;
  int mblk = xcd * 4 + (idx & 3), nblk = idx >> 2;
  const int m0 = mblk * 64;
  f32x4 acc[G2C::MB][G2C::NB];
#pragma unroll
  for (int i = 0; i < G2C::MB; ++i)
#pragma unroll
    for (int j = 0; j < G2C::NB; ++j) acc[i][j] = (f32x4){0, 0, 0, 0};
  auto pre = [&]() {
    if (LIN) {
      f32x4 v4 = *(const f32x4*)(zsum + (long)m0 * 32 + tid * 4);
      float* dst = sinv + (tid >> 3) * 33 + (tid & 7) * 4;
#pragma unroll
      for (int j = 0; j < 4; ++j) dst[j] = v4[j];
    }
  };
  gemm_mainloop<64, 2, 2, 4, 4, LIN>(pre, sinv, a, D_, w, D_, m0, nblk * 128, 64, smem, acc);

  const int wid = tid >> 6, wm = wid / 4, wn = wid % 4;
  float* T = (float*)smem;
  constexpr int TP = 132;
  const int l15 = lane & 15, g = lane >> 4;
#pragma unroll
  for (int mb = 0; mb < G2C::MB; ++mb)
#pragma unroll
    for (int nb = 0; nb < G2C::NB; ++nb)
      *(f32x4*)(T + (wm * 32 + mb * 16 + l15) * TP + wn * 32 + nb * 16 + g * 4) = acc[mb][nb];
  __syncthreads();
  int row = tid >> 3, part = tid & 7;
  const u16* xo = xold + (long)(m0 + row) * D_ + nblk * 128 + part * 16;
  u16* xn = xnew + (long)(m0 + row) * D_ + nblk * 128 + part * 16;
  float v[16];
  float s = 0;
#pragma unroll
  for (int i = 0; i < 2; ++i) {
    bf16x8 xv = *(const bf16x8*)(xo + i * 8);
    u32x4 o;
#pragma unroll
    for (int j = 0; j < 4; ++j) {
      float f0 = bfr(bf2f((u16)xv[2 * j]) + T[row * TP + part * 16 + i * 8 + 2 * j]);
      float f1 = bfr(bf2f((u16)xv[2 * j + 1]) + T[row * TP + part * 16 + i * 8 + 2 * j + 1]);
      v[i * 8 + 2 * j] = f0; v[i * 8 + 2 * j + 1] = f1;
      s += f0 + f1;
      o[j] = pack2(f0, f1);
    }
    *(u32x4*)(xn + i * 8) = o;
  }
  s += __shfl_xor(s, 1); s += __shfl_xor(s, 2); s += __shfl_xor(s, 4);
  float mu = s * (1.f / 128.f), m2 = 0;
#pragma unroll
  for (int j = 0; j < 16; ++j) { float d = v[j] - mu; m2 += d * d; }
  m2 += __shfl_xor(m2, 1); m2 += __shfl_xor(m2, 2); m2 += __shfl_xor(m2, 4);
  if (part == 0) stats[(long)(m0 + row) * NPART + nblk] = make_float2(s, m2);
}

extern "C" void kernel_launch(void* const* d_in, const int* in_sizes, int n_in, void* d_out, int out_size,
                              void* d_ws, size_t ws_size, hipStream_t stream) {
  const u16* x_in = (const u16*)d_in[0];
  const u16* A_wqkv = (const u16*)d_in[1];
  const u16* A_wo = (const u16*)d_in[2];
  const u16* B_wqkv = (const u16*)d_in[4];
  const u16* B_wo = (const u16*)d_in[5];
  u16* out = (u16*)d_out;
  char* ws = (char*)d_ws;
  size_t off = 0;
  auto take = [&](size_t bytes) { char* p = ws + off; off += (bytes + 255) & ~(size_t)255; return p; };
  u16* xb0 = (u16*)take((size_t)S_ * D_ * 2);
  u16* xb1 = (u16*)take((size_t)S_ * D_ * 2);
  u16* qkv = (u16*)take((size_t)S_ * N3 * 2);
  u16* att = (u16*)take((size_t)S_ * D_ * 2);
  u16* Mm = (u16*)take((size_t)D_ * D_ * 2);
  float* kvp = (float*)take((size_t)16 * H_ * 1024 * 4);
  float* zp = (float*)take((size_t)16 * D_ * 4);
  float2* stats = (float2*)take((size_t)S_ * NPART * 8);
  float* csA = (float*)take((size_t)CSP * N3 * 4);
  float* csB = (float*)take((size_t)CSP * N3 * 4);
  float* zsum = (float*)take((size_t)S_ * 32 * 4);

  k_init<<<S_ / 4 + 2 * 12 * CSP, 256, 0, stream>>>(x_in, stats, A_wqkv, B_wqkv, csA, csB);
  k_qkv<0><<<256, 512, 0, stream>>>(x_in, A_wqkv, stats, csA, att, kvp, zp);
  k_lin_mid<<<384, 256, 0, stream>>>(kvp, zp, A_wo, Mm, att, zsum);
  k_out<true><<<256, 512, 0, stream>>>(att, Mm, x_in, xb0, stats, zsum);
  k_qkv<1><<<256, 512, 0, stream>>>(xb0, B_wqkv, stats, csB, qkv, kvp, zp);
  k_swa<<<512, 256, 0, stream>>>(qkv, att);
  k_out<false><<<256, 512, 0, stream>>>(att, B_wo, xb0, xb1, stats, zsum);
  k_qkv<0><<<256, 512, 0, stream>>>(xb1, A_wqkv, stats, csA, att, kvp, zp);
  k_lin_mid<<<384, 256, 0, stream>>>(kvp, zp, A_wo, Mm, att, zsum);
  k_out<true><<<256, 512, 0, stream>>>(att, Mm, xb1, xb0, stats, zsum);
  k_qkv<1><<<256, 512, 0, stream>>>(xb0, B_wqkv, stats, csB, qkv, kvp, zp);
  k_swa<<<512, 256, 0, stream>>>(qkv, att);
  k_out<false><<<256, 512, 0, stream>>>(att, B_wo, xb0, out, stats, zsum);
}
```

```cpp
#include <hip/hip_runtime.h>
#include <hip/hip_bf16.h>
#include <stdint.h>
#include <type_traits>

constexpr int S_ = 2048, D_ = 1024, H_ = 32, DH = 32, WIN = 256;
constexpr int N3 = 3 * D_;
constexpr int NPART = 8;
constexpr int CSP = 8;

typedef __attribute__((ext_vector_type(8))) short bf16x8;
typedef __attribute__((ext_vector_type(4))) short s16x4;
typedef __attribute__((ext_vector_type(4))) float f32x4;
typedef __attribute__((ext_vector_type(16))) float f32x16;
typedef __attribute__((ext_vector_type(4))) unsigned u32x4;
typedef __attribute__((ext_vector_type(2))) unsigned u32x2;
typedef __attribute__((address_space(3))) s16x4* lds_s16x4_p;
typedef unsigned short u16;

#define DEV __device__ __forceinline__

DEV float bf2f(u16 v) { return __uint_as_float(((unsigned)v) << 16); }
typedef __attribute__((ext_vector_type(2))) float f32x2_t;
typedef __attribute__((ext_vector_type(2))) __bf16 bf16x2_t;
DEV unsigned pack2(float lo, float hi) {
  f32x2_t v = {lo, hi};
  bf16x2_t b = __builtin_convertvector(v, bf16x2_t);
  return __builtin_bit_cast(unsigned, b);
}
DEV u16 f2bf(float f) { return (u16)(pack2(f, f) & 0xffffu); }
DEV float bfr(float f) { return __uint_as_float(pack2(f, f) << 16); }
DEV float phi_f(float t) { return (t > 0.f ? t + 1.f : __expf(t)) + 1e-6f; }

DEV s16x4 tr_read(const char* p) {
  return __builtin_amdgcn_ds_read_tr16_b64_v4i16((lds_s16x4_p)(p));
}
DEV bf16x8 cat8(s16x4 a, s16x4 b) { return __builtin_shufflevector(a, b, 0, 1, 2, 3, 4, 5, 6, 7); }

#define WAITV(n) asm volatile("s_waitcnt vmcnt(" #n ")" ::: "memory")
template <int N> DEV void waitv() { asm volatile("s_waitcnt vmcnt(%0)" ::"n"(N) : "memory"); }
DEV void wait_lgkm0() { __builtin_amdgcn_s_waitcnt(0xC07F); }
DEV void raw_barrier() {
  asm volatile("" ::: "memory");
  wait_lgkm0();
  __builtin_amdgcn_s_barrier();
  asm volatile("" ::: "memory");
}

template <int BM, int NSEG, int WGM, int WGN, int NSTAGE>
struct GemmCfg {
  static constexpr int NW = WGM * WGN;
  static constexpr int NT_ = NW * 64;
  static constexpr int BN = NSEG * 64;
  static constexpr int WTM = BM / WGM, WTN = BN / WGN;
  static constexpr int MB = WTM / 16, NB = WTN / 16;
  static constexpr int APIECES = BM / 8, BPIECES = NSEG * 8;
  static constexpr int PPW = (APIECES + BPIECES) / NW;
  static constexpr int STAGE_BYTES = (APIECES + BPIECES) * 1024;
  static constexpr int LDS_BYTES = STAGE_BYTES * NSTAGE;
  static_assert((APIECES + BPIECES) % NW == 0, "pieces");
};

template <int BM, int NSEG, int WGM, int WGN, int NSTAGE, bool SCALE, typename PRE>
DEV void gemm_mainloop(PRE pre, const float* sinv  , const u16* __restrict__ A, int lda, const u16* __restrict__ B, int ldb,
                       int m0, int colbase0, int segstride, char* smem,
                       f32x4 (&acc)[GemmCfg<BM, NSEG, WGM, WGN, NSTAGE>::MB][GemmCfg<BM, NSEG, WGM, WGN, NSTAGE>::NB]) {
  using C = GemmCfg<BM, NSEG, WGM, WGN, NSTAGE>;
  constexpr int KT = D_ / 64;
  const int tid = threadIdx.x, lane = tid & 63;
  const int wid = __builtin_amdgcn_readfirstlane(tid >> 6);
  const int wm = wid / WGN, wn = wid % WGN;

  const u16* gp[C::PPW];
  int gstep[C::PPW];

#pragma unroll
  for (int i = 0; i < C::PPW; ++i) {
    int p = wid * C::PPW + i;
    if (p < C::APIECES) {
      int row = p * 8 + (lane >> 3), slot = lane & 7;
      int chunk = slot ^ ((row >> 1) & 7);
      gstep[i] = 64;
      gp[i] = A + (long)(m0 + row) * lda + chunk * 8;
    } else {
      int pb = p - C::APIECES;
      int seg = pb >> 3, k = (pb & 7) * 8 + (lane >> 3), slot = lane & 7;
      int f = ((k >> 1) & 1) | (((k >> 3) & 1) << 1);
      int chunk = slot ^ (f << 1);
      int cb = colbase0 + seg * segstride;
      gstep[i] = 64 * ldb;
      gp[i] = B + (long)k * ldb + cb + chunk * 8;
    }
  }
  auto issue_piece = [&](int stage, int i) {
    char* base = smem + stage * C::STAGE_BYTES + wid * C::PPW * 1024;
    __builtin_amdgcn_global_load_lds((const unsigned*)gp[i], (unsigned*)(base + i * 1024), 16, 0, 0);
    gp[i] += gstep[i];
  };

  const int l15 = lane & 15, g = lane >> 4;
  const int a_off = (wm * C::WTM + l15) * 128;
  const int a_sw = (l15 >> 1);
  const int q = (lane & 15) >> 2, p = lane & 3;
  const int fB = ((q >> 1) & 1) | ((g & 1) << 1);
  const int b_base = C::APIECES * 1024 + (8 * g + q) * 128 + (p & 1) * 8;

#pragma unroll
  for (int s = 0; s < NSTAGE; ++s)
#pragma unroll
    for (int i = 0; i < C::PPW; ++i) issue_piece(s, i);
  pre();

  bf16x8 af[2][C::MB];
  s16x4 bl[2][C::NB][2];
  constexpr int NM = C::NB * C::MB;
  constexpr int NRD = C::NB * 2 + C::MB;
  auto read1 = [&](int stage, int ks, int set, int k) {
    const char* sb = smem + stage * C::STAGE_BYTES;
    if (k < C::NB * 2) {
      int nb = k >> 1, hf = k & 1;
      int gnb = wn * C::NB + nb;
      int seg = gnb >> 2, c = gnb & 3;
      const char* bp = sb + b_base + seg * 8192 + ks * 4096 + ((((2 * c + (p >> 1)) ^ (fB << 1))) << 4);
      bl[set][nb][hf] = tr_read(bp + hf * 512);
    } else {
      int mb = k - C::NB * 2;
      af[set][mb] = *(const bf16x8*)(sb + a_off + mb * 2048 + (((ks * 4 + g) ^ a_sw) << 4));
    }
  };
  auto phase = [&](int set, bool rd, int rstage, int rks, bool iss, int dstage, int head) {
    float scl[C::MB];
    f32x4 t4[C::MB][C::NB];
    if (SCALE) {
      asm volatile("" ::: "memory");
#pragma unroll
      for (int mb = 0; mb < C::MB; ++mb) scl[mb] = sinv[(wm * C::WTM + mb * 16 + l15) * 33 + head];
    }
    constexpr int NSIDE_MAX = NRD + C::PPW;
    constexpr int PER = (NSIDE_MAX + NM - 1) / NM;
    const int nrd = rd ? NRD : 0;
    const int nside = nrd + (iss ? C::PPW : 0);
#pragma unroll
    for (int nb = 0; nb < C::NB; ++nb)
#pragma unroll
      for (int mb = 0; mb < C::MB; ++mb) {
        const int idx = nb * C::MB + mb;
        if (SCALE) {
          t4[mb][nb] = __builtin_amdgcn_mfma_f32_16x16x32_bf16(cat8(bl[set][nb][0], bl[set][nb][1]), af[set][mb], (f32x4){0, 0, 0, 0}, 0, 0, 0);
        } else {
          acc[mb][nb] = __builtin_amdgcn_mfma_f32_16x16x32_bf16(cat8(bl[set][nb][0], bl[set][nb][1]), af[set][mb], acc[mb][nb], 0, 0, 0);
        }
        __builtin_amdgcn_sched_barrier(0);
#pragma unroll
        for (int e = 0; e < PER; ++e) {
          const int k = idx * PER + e;
          if (k < nrd) read1(rstage, rks, set ^ 1, k);
          else if (k < nside) issue_piece(dstage, k - nrd);
        }
        __builtin_amdgcn_sched_barrier(0);
      }
    if (SCALE) {
#pragma unroll
      for (int nb = 0; nb < C::NB; ++nb)
#pragma unroll
        for (int mb = 0; mb < C::MB; ++mb) {
#pragma unroll
          for (int r = 0; r < 4; ++r) acc[mb][nb][r] = fmaf(t4[mb][nb][r], scl[mb], acc[mb][nb][r]);
          asm volatile("" : "+v"(acc[mb][nb]));
        }
    }
  };
  const bool grpA = wid < C::NW / 2;

  waitv<C::PPW*(NSTAGE - 1)>();
  raw_barrier();
#pragma unroll
  for (int k = 0; k < NRD; ++k) read1(0, 0, 0, k);
  auto run = [&](auto GA) {
    constexpr bool ga = decltype(GA)::value;
#pragma unroll
    for (int kt = 0; kt < KT; ++kt) {
      const int cs = kt % NSTAGE, ns = (kt + 1) % NSTAGE, ps = (kt + NSTAGE - 1) % NSTAGE;
      __builtin_amdgcn_sched_barrier(0);
      phase(0, true, cs, 1, ga && kt >= 1 && kt - 1 + NSTAGE < KT, ps, 2 * kt);
      if (kt + 1 < KT) {
        if (kt + NSTAGE - 1 < KT) waitv<C::PPW*(NSTAGE - 2)>(); else waitv<0>();
        raw_barrier();
        __builtin_amdgcn_sched_barrier(0);
        phase(1, true, ns, 0, !ga && kt + NSTAGE < KT, cs, 2 * kt + 1);
      } else {
        phase(1, false, 0, 0, false, 0, 2 * kt + 1);
      }
    }
    __builtin_amdgcn_sched_barrier(0);
  };
  if (grpA) run(std::integral_constant<bool, true>{}); else run(std::integral_constant<bool, false>{});
  raw_barrier();
}

__global__ __launch_bounds__(256) void k_init(const u16* __restrict__ x, float2* __restrict__ stats,
                                              const u16* __restrict__ wA, const u16* __restrict__ wB,
                                              float* __restrict__ csA, float* __restrict__ csB) {
  const int tid = threadIdx.x;
  if (blockIdx.x < S_ / 4) {
    int row = blockIdx.x * 4 + (tid >> 6), lane = tid & 63;
    const bf16x8* px = (const bf16x8*)(x + (long)row * D_ + lane * 16);
    bf16x8 v0 = px[0], v1 = px[1];
    float f[16];
#pragma unroll
    for (int j = 0; j < 8; ++j) { f[j] = bf2f((u16)v0[j]); f[8 + j] = bf2f((u16)v1[j]); }
    float s = 0;
#pragma unroll
    for (int j = 0; j < 16; ++j) s += f[j];
    s += __shfl_xor(s, 1); s += __shfl_xor(s, 2); s += __shfl_xor(s, 4);
    float mu = s * (1.f / 128.f), m2 = 0;
#pragma unroll
    for (int j = 0; j < 16; ++j) { float d = f[j] - mu; m2 += d * d; }
    m2 += __shfl_xor(m2, 1); m2 += __shfl_xor(m2, 2); m2 += __shfl_xor(m2, 4);
    if ((lane & 7) == 0) stats[row * NPART + (lane >> 3)] = make_float2(s, m2);
  } else {
    int b = blockIdx.x - S_ / 4;
    int wsel = b / (12 * CSP); b %= 12 * CSP;
    int cb = b / CSP, rs = b % CSP;
    const u16* w = wsel ? wB : wA;
    float* cs = wsel ? csB : csA;
    __shared__ float red[8][256];
    int cg = tid & 31, rg = tid >> 5;
    float a[8] = {0, 0, 0, 0, 0, 0, 0, 0};
    bf16x8 v[16];
#pragma unroll
    for (int i = 0; i < 16; ++i) v[i] = *(const bf16x8*)(w + (long)(rs * 128 + rg + i * 8) * N3 + cb * 256 + cg * 8);
#pragma unroll
    for (int i = 0; i < 16; ++i)
#pragma unroll
      for (int j = 0; j < 8; ++j) a[j] += bf2f((u16)v[i][j]);
#pragma unroll
    for (int j = 0; j < 8; ++j) red[rg][cg * 8 + j] = a[j];
    __syncthreads();
    float t = 0;
#pragma unroll
    for (int i = 0; i < 8; ++i) t += red[i][tid];
    cs[rs * N3 + cb * 256 + tid] = t;
  }
}

using G1C = GemmCfg<128, 3, 2, 4, 3>;
constexpr int G1_TP = 200;

template <int MODE>
__global__ __launch_bounds__(512, 2) void k_qkv(const u16* __restrict__ x, const u16* __restrict__ w,
                                                const float2* __restrict__ stats, const float* __restrict__ csp,
                                                u16* __restrict__ out  , float* __restrict__ kvp,
                                                float* __restrict__ zp) {
  __shared__ __attribute__((aligned(16))) char smem[G1C::LDS_BYTES + 2048];
  float* s_mean = (float*)(smem + G1C::LDS_BYTES);
  float* s_rstd = s_mean + 128;
  float* s_cs = s_rstd + 128;
  const int tid = threadIdx.x, lane = tid & 63;
  int bid = blockIdx.x;
  int xcd = bid & 7, idx = bid >> 3;
  int mblk = (xcd & 1) * 8 + (idx & 7), nblk = (xcd >> 1) * 4 + (idx >> 3);
  const int m0 = mblk * 128;

  f32x4 acc[G1C::MB][G1C::NB];
#pragma unroll
  for (int i = 0; i < G1C::MB; ++i)
#pragma unroll
    for (int j = 0; j < G1C::NB; ++j) acc[i][j] = (f32x4){0, 0, 0, 0};

  auto pre = [&]() {
  if (tid < 128) {
    const float2* sp = stats + (long)(m0 + tid) * NPART;
    float2 pr[NPART];
#pragma unroll
    for (int i = 0; i < NPART; ++i) pr[i] = sp[i];
    float s = 0;
#pragma unroll
    for (int i = 0; i < NPART; ++i) s += pr[i].x;
    float mean = s * (1.f / D_), m2 = 0;
#pragma unroll
    for (int i = 0; i < NPART; ++i) { float d = pr[i].x * (1.f / 128.f) - mean; m2 += pr[i].y + 128.f * d * d; }
    s_mean[tid] = mean;
    s_rstd[tid] = rsqrtf(m2 * (1.f / D_) + 1e-5f);
  } else if (tid < 128 + 192) {
    int c = tid - 128;
    int col = (c >> 6) * D_ + nblk * 64 + (c & 63);
    float t = 0;
#pragma unroll
    for (int i = 0; i < CSP; ++i) t += csp[i * N3 + col];
    s_cs[c] = t;
  }

  };
  gemm_mainloop<128, 3, 2, 4, 3, false>(pre, nullptr, x, D_, w, N3, m0, nblk * 64, D_, smem, acc);

  const int wid = tid >> 6, wm = wid / 4, wn = wid % 4;
  u16* T = (u16*)smem;
  const int l15 = lane & 15, g = lane >> 4;
#pragma unroll
  for (int mb = 0; mb < G1C::MB; ++mb) {
    const int row = wm * G1C::WTM + mb * 16 + l15;
    const float mean = s_mean[row], rstd = s_rstd[row];
#pragma unroll
    for (int nb = 0; nb < G1C::NB; ++nb) {
      const int col = wn * G1C::WTN + nb * 16 + g * 4;
      const f32x4 csv = *(const f32x4*)(s_cs + col);
      const int seg = col >> 6;
      float y[4];
#pragma unroll
      for (int r = 0; r < 4; ++r) {
        y[r] = (acc[mb][nb][r] - mean * csv[r]) * rstd;
        if (MODE == 0 && seg < 2) y[r] = phi_f(bfr(y[r]));
      }
      u32x2 w2;
      w2[0] = pack2(y[0], y[1]); w2[1] = pack2(y[2], y[3]);
      *(u32x2*)(T + row * G1_TP + col) = w2;
    }
  }
  __syncthreads();
  if (MODE == 1) {
#pragma unroll
    for (int i = 0; i < 6; ++i) {
      const int row = tid >> 2, part = tid & 3;
      u32x4 v = *(const u32x4*)(T + row * G1_TP + i * 32 + part * 8);
      *(u32x4*)(out + ((long)((i >> 1) * H_ + nblk * 2 + (i & 1)) * S_ + m0 + row) * DH + part * 8) = v;
    }
  } else {
#pragma unroll
    for (int i = 0; i < 2; ++i) {
      int ch = tid + i * 512;
      int row = ch >> 3, c = ch & 7;
      u32x4 v = *(const u32x4*)(T + row * G1_TP + c * 8);
      *(u32x4*)(out + (long)(m0 + row) * D_ + nblk * 64 + c * 8) = v;
    }
    int hh = wid >> 2, dt = (wid >> 1) & 1, et = wid & 1;
    int q = (lane & 15) >> 2, p = lane & 3;
    f32x4 kv = {0, 0, 0, 0}, zz = {0, 0, 0, 0};
    bf16x8 ones;
#pragma unroll
    for (int j = 0; j < 8; ++j) ones[j] = (short)0x3F80;
#pragma unroll
    for (int ks = 0; ks < 4; ++ks) {
      int r0 = ks * 32 + 8 * g + q;
      const char* pa = (const char*)(T + r0 * G1_TP + 64 + hh * 32 + dt * 16 + 4 * p);
      const char* pb = (const char*)(T + r0 * G1_TP + 128 + hh * 32 + et * 16 + 4 * p);
      bf16x8 a = cat8(tr_read(pa), tr_read(pa + 4 * G1_TP * 2));
      bf16x8 b = cat8(tr_read(pb), tr_read(pb + 4 * G1_TP * 2));
      kv = __builtin_amdgcn_mfma_f32_16x16x32_bf16(a, b, kv, 0, 0, 0);
      zz = __builtin_amdgcn_mfma_f32_16x16x32_bf16(a, ones, zz, 0, 0, 0);
    }
    int head = nblk * 2 + hh;
    float* kp = kvp + ((long)(mblk * H_ + head) * 32) * 32;
#pragma unroll
    for (int r = 0; r < 4; ++r) {
      int d = dt * 16 + g * 4 + r;
      kp[d * 32 + et * 16 + l15] = kv[r];
      if (et == 0 && l15 == 0) zp[mblk * D_ + head * 32 + d] = zz[r];
    }
  }
}

__global__ __launch_bounds__(256) void k_lin_mid(const float* __restrict__ kvp, const float* __restrict__ zp,
                                                 const u16* __restrict__ wo, u16* __restrict__ M,
                                                 u16* __restrict__ pq  ) {
  constexpr int WP = 264;
  constexpr int KP = 40;
  __shared__ __attribute__((aligned(16))) u16 sw[32 * WP];
  __shared__ __attribute__((aligned(16))) u16 skv[32 * KP];
  const int tid = threadIdx.x, lane = tid & 63, wid = tid >> 6;
  if (blockIdx.x >= 128) {
    const int r0 = (blockIdx.x - 128) * 8 + wid * 2;
    float* zs = (float*)sw;
    f32x4 zpr[16];
#pragma unroll
    for (int mb = 0; mb < 16; ++mb) zpr[mb] = *(const f32x4*)(zp + mb * D_ + tid * 4);
    bf16x8 v[2][2];
#pragma unroll
    for (int r = 0; r < 2; ++r) {
      const u16* pp = pq + (long)(r0 + r) * D_ + lane * 16;
      v[r][0] = *(const bf16x8*)(pp);
      v[r][1] = *(const bf16x8*)(pp + 8);
    }
    f32x4 zs4 = {0, 0, 0, 0};
#pragma unroll
    for (int mb = 0; mb < 16; ++mb) zs4 += zpr[mb];
    *(f32x4*)(zs + tid * 4) = zs4;
    __syncthreads();
    f32x4 z4[4];
#pragma unroll
    for (int i = 0; i < 4; ++i) z4[i] = *(const f32x4*)(zs + lane * 16 + i * 4);
#pragma unroll
    for (int r = 0; r < 2; ++r) {
      float den = 0;
#pragma unroll
      for (int i = 0; i < 2; ++i)
#pragma unroll
        for (int j = 0; j < 4; ++j)
          den += bf2f((u16)v[r][i][j]) * z4[2 * i][j] + bf2f((u16)v[r][i][4 + j]) * z4[2 * i + 1][j];
      den += __shfl_xor(den, 1);
      const float iv = 1.f / (den + 1e-6f);
      u16* pp = pq + (long)(r0 + r) * D_ + lane * 16;
#pragma unroll
      for (int i = 0; i < 2; ++i) {
        u32x4 o;
#pragma unroll
        for (int j = 0; j < 4; ++j) o[j] = pack2(bf2f((u16)v[r][i][2 * j]) * iv, bf2f((u16)v[r][i][2 * j + 1]) * iv);
        *(u32x4*)(pp + i * 8) = o;
      }
    }
    return;
  }
  const int h = blockIdx.x >> 2, nq = blockIdx.x & 3;
  f32x4 pr[16];
#pragma unroll
  for (int mb = 0; mb < 16; ++mb) pr[mb] = *(const f32x4*)(kvp + ((long)(mb * H_ + h) * 1024) + tid * 4);
  u32x4 wv[4];
#pragma unroll
  for (int i = 0; i < 4; ++i)
    wv[i] = *(const u32x4*)(wo + (long)(h * 32 + (tid >> 5) + i * 8) * D_ + nq * 256 + (tid & 31) * 8);
  f32x4 s4 = {0, 0, 0, 0};
#pragma unroll
  for (int mb = 0; mb < 16; ++mb) s4 += pr[mb];
  {
    u32x2 w2;
    w2[0] = pack2(s4[0], s4[1]); w2[1] = pack2(s4[2], s4[3]);
    *(u32x2*)(skv + (tid >> 3) * KP + (tid & 7) * 4) = w2;
  }
#pragma unroll
  for (int i = 0; i < 4; ++i) *(u32x4*)(sw + ((tid >> 5) + i * 8) * WP + (tid & 31) * 8) = wv[i];
  __syncthreads();
  const int l15 = lane & 15, g = lane >> 4, q = (lane & 15) >> 2, p = lane & 3;
  bf16x8 kf[2];
#pragma unroll
  for (int db = 0; db < 2; ++db) kf[db] = *(const bf16x8*)(skv + (db * 16 + l15) * KP + g * 8);
#pragma unroll
  for (int nb = 0; nb < 4; ++nb) {
    const int n0 = wid * 64 + nb * 16;
    const char* pa = (const char*)(sw + (8 * g + q) * WP + n0 + 4 * p);
    bf16x8 wf = cat8(tr_read(pa), tr_read(pa + 4 * WP * 2));
#pragma unroll
    for (int db = 0; db < 2; ++db) {
      f32x4 d4 = __builtin_amdgcn_mfma_f32_16x16x32_bf16(wf, kf[db], (f32x4){0, 0, 0, 0}, 0, 0, 0);
      u32x2 w2;
      w2[0] = pack2(d4[0], d4[1]); w2[1] = pack2(d4[2], d4[3]);
      *(u32x2*)(M + (long)(h * 32 + db * 16 + l15) * D_ + nq * 256 + n0 + 4 * g) = w2;
    }
  }
}

DEV int crow(int r, int hi) { return (r & 3) + 8 * (r >> 2) + 4 * hi; }

__global__ __launch_bounds__(256, 2) void k_swa(const u16* __restrict__ qkv, u16* __restrict__ att) {
  __shared__ __attribute__((aligned(16))) char sK[384 * 64];
  __shared__ __attribute__((aligned(16))) char sV[384 * 64];
  const int tid = threadIdx.x, lane = tid & 63, wid = tid >> 6;
  const int h = blockIdx.x & 31, qb = blockIdx.x >> 5;
  const int q0 = qb * 128, kbase = q0 - 256;
#pragma unroll
  for (int i = 0; i < 6; ++i) {
    int ch = tid + i * 256;
    int r = ch >> 2, c = ch & 3;
    int key = kbase + r;
    u32x4 kv4 = {0, 0, 0, 0}, vv4 = {0, 0, 0, 0};
    if (key >= 0) {
      kv4 = *(const u32x4*)(qkv + ((long)(H_ + h) * S_ + key) * DH + c * 8);
      vv4 = *(const u32x4*)(qkv + ((long)(2 * H_ + h) * S_ + key) * DH + c * 8);
    }
    *(u32x4*)(sK + r * 64 + ((c ^ ((r >> 2) & 3)) << 4)) = kv4;
    *(u32x4*)(sV + r * 64 + (c << 4)) =vv4;
  }
  const int l31 = lane & 31, hi = lane >> 5;
  const int qw = q0 + wid * 32;
  const int qi = qw + l31;
  bf16x8 qf[2];
#pragma unroll
  for (int ks = 0; ks < 2; ++ks) qf[ks] = *(const bf16x8*)(qkv + ((long)h * S_ + qi) * DH + ks * 16 + hi * 8);
  __syncthreads();

  typedef __attribute__((ext_vector_type(2))) float f32x2;
  const int kt0 = (qw >= 256) ? 0 : (256 - qw) / 32;
  f32x16 sc[9];
  const float scale_l2 = 0.17677669529663687f * 1.4426950408889634f;
  float mx = -3.0e38f;
  const f32x16 zero16 = {0, 0, 0, 0, 0, 0, 0, 0, 0, 0, 0, 0, 0, 0, 0, 0};
#pragma unroll
  for (int kt = 0; kt < 9; ++kt) {
    f32x16 a16;
    if (kt >= kt0) {
      int r = wid * 32 + kt * 32 + l31;
      bf16x8 kf0 = *(const bf16x8*)(sK + r * 64 + ((hi ^ ((r >> 2) & 3)) << 4));
      bf16x8 kf1 = *(const bf16x8*)(sK + r * 64 + (((2 + hi) ^ ((r >> 2) & 3)) << 4));
      a16 = __builtin_amdgcn_mfma_f32_32x32x16_bf16(kf0, qf[0], zero16, 0, 0, 0);
      a16 = __builtin_amdgcn_mfma_f32_32x32x16_bf16(kf1, qf[1], a16, 0, 0, 0);
      if (kt == 0) {
#pragma unroll
        for (int r2 = 0; r2 < 16; ++r2) a16[r2] = (crow(r2, hi) > l31) ? a16[r2] : -3.0e38f;
      }
      if (kt == 8) {
#pragma unroll
        for (int r2 = 0; r2 < 16; ++r2) a16[r2] = (crow(r2, hi) <= l31) ? a16[r2] : -3.0e38f;
      }
#pragma unroll
      for (int r2 = 0; r2 < 16; r2 += 2) mx = fmaxf(fmaxf(mx, a16[r2]), a16[r2 + 1]);
    } else {
#pragma unroll
      for (int i = 0; i < 16; ++i) a16[i] = -3.0e38f;
    }
    sc[kt] = a16;
  }
  mx = fmaxf(mx, __shfl_xor(mx, 32));
  const float mxs = -mx * scale_l2;
  const f32x2 sc2 = {scale_l2, scale_l2}, mx2 = {mxs, mxs};
  f32x2 sum2 = {0, 0};
#pragma unroll
  for (int kt = 0; kt < 9; ++kt)
#pragma unroll
    for (int r = 0; r < 16; r += 2) {
      f32x2 t = {sc[kt][r], sc[kt][r + 1]};
      t = t * sc2 + mx2;
      f32x2 e = {__builtin_amdgcn_exp2f(t[0]), __builtin_amdgcn_exp2f(t[1])};
      sc[kt][r] = e[0]; sc[kt][r + 1] = e[1];
      sum2 += e;
    }
  float sum = sum2[0] + sum2[1];
  sum += __shfl_xor(sum, 32);
  f32x16 o = zero16;
  const int q4 = (lane & 15) >> 2, p4 = lane & 3, blk = (lane >> 4) & 1;
#pragma unroll
  for (int kt = 0; kt < 9; ++kt) {
#pragma unroll
    for (int s = 0; s < 2; ++s) {
      u32x4 pk;
#pragma unroll
      for (int j = 0; j < 4; ++j) pk[j] = pack2(sc[kt][8 * s + 2 * j], sc[kt][8 * s + 2 * j + 1]);
      bf16x8 pf = __builtin_bit_cast(bf16x8, pk);
      int r0 = wid * 32 + kt * 32 + 16 * s + 4 * hi + q4;
      const char* vp = sV + r0 * 64 + (16 * blk + 4 * p4) * 2;
      bf16x8 vf = cat8(tr_read(vp), tr_read(vp + 8 * 64));
      o = __builtin_amdgcn_mfma_f32_32x32x16_bf16(vf, pf, o, 0, 0, 0);
    }
  }
  float inv = 1.f / sum;
  u16* op = att + (long)qi * D_ + h * 32;
#pragma unroll
  for (int gq = 0; gq < 4; ++gq) {
    u32x2 w2;
    w2[0] = pack2(o[4 * gq + 0] * inv, o[4 * gq + 1] * inv);
    w2[1] = pack2(o[4 * gq + 2] * inv, o[4 * gq + 3] * inv);
    *(u32x2*)(op + 8 * gq + 4 * hi) = w2;
  }
}

using G2C = GemmCfg<64, 2, 2, 4, 4>;

template <bool LIN>
__global__ __launch_bounds__(512, 2) void k_out(const u16* __restrict__ a, const u16* __restrict__ w,
                                                const u16* __restrict__ xold, u16* __restrict__ xnew,
                                                float2* __restrict__ stats, const float* __restrict__ zsum) {
  __shared__ __attribute__((aligned(16))) char smem[G2C::LDS_BYTES + 64 * 33 * 4];
  float* sinv = (float*)(smem + G2C::LDS_BYTES);
  const int tid = threadIdx.x, lane = tid & 63;
  int bid = blockIdx.x;
  int xcd = bid & 7, idx = bid >> 3;
  int mblk = xcd * 4 + (idx & 3), nblk = idx >> 2;
  const int m0 = mblk * 64;
  f32x4 acc[G2C::MB][G2C::NB];
#pragma unroll
  for (int i = 0; i < G2C::MB; ++i)
#pragma unroll
    for (int j = 0; j < G2C::NB; ++j) acc[i][j] = (f32x4){0, 0, 0, 0};
  auto pre = [&]() {
    if (LIN) {
      f32x4 v4 = *(const f32x4*)(zsum + (long)m0 * 32 + tid * 4);
      float* dst = sinv + (tid >> 3) * 33 + (tid & 7) * 4;
#pragma unroll
      for (int j = 0; j < 4; ++j) dst[j] = v4[j];
    }
  };
  gemm_mainloop<64, 2, 2, 4, 4, LIN>(pre, sinv, a, D_, w, D_, m0, nblk * 128, 64, smem, acc);

  const int wid = tid >> 6, wm = wid / 4, wn = wid % 4;
  float* T = (float*)smem;
  constexpr int TP = 132;
  const int l15 = lane & 15, g = lane >> 4;
#pragma unroll
  for (int mb = 0; mb < G2C::MB; ++mb)
#pragma unroll
    for (int nb = 0; nb < G2C::NB; ++nb)
      *(f32x4*)(T + (wm * 32 + mb * 16 + l15) * TP + wn * 32 + nb * 16 + g * 4) = acc[mb][nb];
  __syncthreads();
  int row = tid >> 3, part = tid & 7;
  const u16* xo = xold + (long)(m0 + row) * D_ + nblk * 128 + part * 16;
  u16* xn = xnew + (long)(m0 + row) * D_ + nblk * 128 + part * 16;
  float v[16];
  float s = 0;
#pragma unroll
  for (int i = 0; i < 2; ++i) {
    bf16x8 xv = *(const bf16x8*)(xo + i * 8);
    u32x4 o;
#pragma unroll
    for (int j = 0; j < 4; ++j) {
      float f0 = bfr(bf2f((u16)xv[2 * j]) + T[row * TP + part * 16 + i * 8 + 2 * j]);
      float f1 = bfr(bf2f((u16)xv[2 * j + 1]) + T[row * TP + part * 16 + i * 8 + 2 * j + 1]);
      v[i * 8 + 2 * j] = f0; v[i * 8 + 2 * j + 1] = f1;
      s += f0 + f1;
      o[j] = pack2(f0, f1);
    }
    *(u32x4*)(xn + i * 8) = o;
  }
  s += __shfl_xor(s, 1); s += __shfl_xor(s, 2); s += __shfl_xor(s, 4);
  float mu = s * (1.f / 128.f), m2 = 0;
#pragma unroll
  for (int j = 0; j < 16; ++j) { float d = v[j] - mu; m2 += d * d; }
  m2 += __shfl_xor(m2, 1); m2 += __shfl_xor(m2, 2); m2 += __shfl_xor(m2, 4);
  if (part == 0) stats[(long)(m0 + row) * NPART + nblk] = make_float2(s, m2);
}

extern "C" void kernel_launch(void* const* d_in, const int* in_sizes, int n_in, void* d_out, int out_size,
                              void* d_ws, size_t ws_size, hipStream_t stream) {
  const u16* x_in = (const u16*)d_in[0];
  const u16* A_wqkv = (const u16*)d_in[1];
  const u16* A_wo = (const u16*)d_in[2];
  const u16* B_wqkv = (const u16*)d_in[4];
  const u16* B_wo = (const u16*)d_in[5];
  u16* out = (u16*)d_out;
  char* ws = (char*)d_ws;
  size_t off = 0;
  auto take = [&](size_t bytes) { char* p = ws + off; off += (bytes + 255) & ~(size_t)255; return p; };
  u16* xb0 = (u16*)take((size_t)S_ * D_ * 2);
  u16* xb1 = (u16*)take((size_t)S_ * D_ * 2);
  u16* qkv = (u16*)take((size_t)S_ * N3 * 2);
  u16* att = (u16*)take((size_t)S_ * D_ * 2);
  u16* Mm = (u16*)take((size_t)D_ * D_ * 2);
  float* kvp = (float*)take((size_t)16 * H_ * 1024 * 4);
  float* zp = (float*)take((size_t)16 * D_ * 4);
  float2* stats = (float2*)take((size_t)S_ * NPART * 8);
  float* csA = (float*)take((size_t)CSP * N3 * 4);
  float* csB = (float*)take((size_t)CSP * N3 * 4);
  float* zsum = (float*)take((size_t)S_ * 32 * 4);

  k_init<<<S_ / 4 + 2 * 12 * CSP, 256, 0, stream>>>(x_in, stats, A_wqkv, B_wqkv, csA, csB);
  k_qkv<0><<<256, 512, 0, stream>>>(x_in, A_wqkv, stats, csA, att, kvp, zp);
  k_lin_mid<<<384, 256, 0, stream>>>(kvp, zp, A_wo, Mm, att);
  k_out<false><<<256, 512, 0, stream>>>(att, Mm, x_in, xb0, stats, zsum);
  k_qkv<1><<<256, 512, 0, stream>>>(xb0, B_wqkv, stats, csB, qkv, kvp, zp);
  k_swa<<<512, 256, 0, stream>>>(qkv, att);
  k_out<false><<<256, 512, 0, stream>>>(att, B_wo, xb0, xb1, stats, zsum);
  k_qkv<0><<<256, 512, 0, stream>>>(xb1, A_wqkv, stats, csA, att, kvp, zp);
  k_lin_mid<<<384, 256, 0, stream>>>(kvp, zp, A_wo, Mm, att);
  k_out<false><<<256, 512, 0, stream>>>(att, Mm, xb1, xb0, stats, zsum);
  k_qkv<1><<<256, 512, 0, stream>>>(xb0, B_wqkv, stats, csB, qkv, kvp, zp);
  k_swa<<<512, 256, 0, stream>>>(qkv, att);
  k_out<false><<<256, 512, 0, stream>>>(att, B_wo, xb0, out, stats, zsum);
}
```

```cpp
#include <hip/hip_runtime.h>
#include <hip/hip_bf16.h>
#include <stdint.h>
#include <type_traits>

constexpr int S_ = 2048, D_ = 1024, H_ = 32, DH = 32, WIN = 256;
constexpr int N3 = 3 * D_;
constexpr int NPART = 32;
constexpr int CSP = 8;

typedef __attribute__((ext_vector_type(8))) short bf16x8;
typedef __attribute__((ext_vector_type(4))) short s16x4;
typedef __attribute__((ext_vector_type(4))) float f32x4;
typedef __attribute__((ext_vector_type(16))) float f32x16;
typedef __attribute__((ext_vector_type(4))) unsigned u32x4;
typedef __attribute__((ext_vector_type(2))) unsigned u32x2;
typedef __attribute__((address_space(3))) s16x4* lds_s16x4_p;
typedef unsigned short u16;

#define DEV __device__ __forceinline__

DEV float bf2f(u16 v) { return __uint_as_float(((unsigned)v) << 16); }
typedef __attribute__((ext_vector_type(2))) float f32x2_t;
typedef __attribute__((ext_vector_type(2))) __bf16 bf16x2_t;
DEV unsigned pack2(float lo, float hi) {
  f32x2_t v = {lo, hi};
  bf16x2_t b = __builtin_convertvector(v, bf16x2_t);
  return __builtin_bit_cast(unsigned, b);
}
DEV u16 f2bf(float f) { return (u16)(pack2(f, f) & 0xffffu); }
DEV float bfr(float f) { return __uint_as_float(pack2(f, f) << 16); }
DEV float phi_f(float t) { return (t > 0.f ? t + 1.f : __expf(t)) + 1e-6f; }

DEV s16x4 tr_read(const char* p) {
  return __builtin_amdgcn_ds_read_tr16_b64_v4i16((lds_s16x4_p)(p));
}
DEV bf16x8 cat8(s16x4 a, s16x4 b) { return __builtin_shufflevector(a, b, 0, 1, 2, 3, 4, 5, 6, 7); }

#define WAITV(n) asm volatile("s_waitcnt vmcnt(" #n ")" ::: "memory")
template <int N> DEV void waitv() { asm volatile("s_waitcnt vmcnt(%0)" ::"n"(N) : "memory"); }
DEV void wait_lgkm0() { __builtin_amdgcn_s_waitcnt(0xC07F); }
DEV void raw_barrier() {
  asm volatile("" ::: "memory");
  wait_lgkm0();
  __builtin_amdgcn_s_barrier();
  asm volatile("" ::: "memory");
}

template <int BM, int NSEG, int WGM, int WGN, int NSTAGE>
struct GemmCfg {
  static constexpr int NW = WGM * WGN;
  static constexpr int NT_ = NW * 64;
  static constexpr int BN = NSEG * 64;
  static constexpr int WTM = BM / WGM, WTN = BN / WGN;
  static constexpr int MB = WTM / 16, NB = WTN / 16;
  static constexpr int APIECES = BM / 8, BPIECES = NSEG * 8;
  static constexpr int PPW = (APIECES + BPIECES) / NW;
  static constexpr int STAGE_BYTES = (APIECES + BPIECES) * 1024;
  static constexpr int LDS_BYTES = STAGE_BYTES * NSTAGE;
  static_assert((APIECES + BPIECES) % NW == 0, "pieces");
};

template <int BM, int NSEG, int WGM, int WGN, int NSTAGE, bool SCALE, typename PRE>
DEV void gemm_mainloop(PRE pre, const float* sinv  , const u16* __restrict__ A, int lda, const u16* __restrict__ B, int ldb,
                       int m0, int colbase0, int segstride, char* smem,
                       f32x4 (&acc)[GemmCfg<BM, NSEG, WGM, WGN, NSTAGE>::MB][GemmCfg<BM, NSEG, WGM, WGN, NSTAGE>::NB]) {
  using C = GemmCfg<BM, NSEG, WGM, WGN, NSTAGE>;
  constexpr int KT = D_ / 64;
  const int tid = threadIdx.x, lane = tid & 63;
  const int wid = __builtin_amdgcn_readfirstlane(tid >> 6);
  const int wm = wid / WGN, wn = wid % WGN;

  const u16* gp[C::PPW];
  int gstep[C::PPW];

#pragma unroll
  for (int i = 0; i < C::PPW; ++i) {
    int p = wid * C::PPW + i;
    if (p < C::APIECES) {
      int row = p * 8 + (lane >> 3), slot = lane & 7;
      int chunk = slot ^ ((row >> 1) & 7);
      gstep[i] = 64;
      gp[i] = A + (long)(m0 + row) * lda + chunk * 8;
    } else {
      int pb = p - C::APIECES;
      int seg = pb >> 3, k = (pb & 7) * 8 + (lane >> 3), slot = lane & 7;
      int f = ((k >> 1) & 1) | (((k >> 3) & 1) << 1);
      int chunk = slot ^ (f << 1);
      int cb = colbase0 + seg * segstride;
      gstep[i] = 64 * ldb;
      gp[i] = B + (long)k * ldb + cb + chunk * 8;
    }
  }
  auto issue_piece = [&](int stage, int i) {
    char* base = smem + stage * C::STAGE_BYTES + wid * C::PPW * 1024;
    __builtin_amdgcn_global_load_lds((const unsigned*)gp[i], (unsigned*)(base + i * 1024), 16, 0, 0);
    gp[i] += gstep[i];
  };

  const int l15 = lane & 15, g = lane >> 4;
  const int a_off = (wm * C::WTM + l15) * 128;
  const int a_sw = (l15 >> 1);
  const int q = (lane & 15) >> 2, p = lane & 3;
  const int fB = ((q >> 1) & 1) | ((g & 1) << 1);
  const int b_base = C::APIECES * 1024 + (8 * g + q) * 128 + (p & 1) * 8;

#pragma unroll
  for (int s = 0; s < NSTAGE; ++s)
#pragma unroll
    for (int i = 0; i < C::PPW; ++i) issue_piece(s, i);
  pre();

  bf16x8 af[2][C::MB];
  s16x4 bl[2][C::NB][2];
  constexpr int NM = C::NB * C::MB;
  constexpr int NRD = C::NB * 2 + C::MB;
  auto read1 = [&](int stage, int ks, int set, int k) {
    const char* sb = smem + stage * C::STAGE_BYTES;
    if (k < C::NB * 2) {
      int nb = k >> 1, hf = k & 1;
      int gnb = wn * C::NB + nb;
      int seg = gnb >> 2, c = gnb & 3;
      const char* bp = sb + b_base + seg * 8192 + ks * 4096 + ((((2 * c + (p >> 1)) ^ (fB << 1))) << 4);
      bl[set][nb][hf] = tr_read(bp + hf * 512);
    } else {
      int mb = k - C::NB * 2;
      af[set][mb] = *(const bf16x8*)(sb + a_off + mb * 2048 + (((ks * 4 + g) ^ a_sw) << 4));
    }
  };
  auto phase = [&](int set, bool rd, int rstage, int rks, bool iss, int dstage, int head) {
    float scl[C::MB];
    f32x4 t4[C::MB][C::NB];
    if (SCALE) {
      asm volatile("" ::: "memory");
#pragma unroll
      for (int mb = 0; mb < C::MB; ++mb) scl[mb] = sinv[(wm * C::WTM + mb * 16 + l15) * 33 + head];
    }
    constexpr int NSIDE_MAX = NRD + C::PPW;
    constexpr int PER = (NSIDE_MAX + NM - 1) / NM;
    const int nrd = rd ? NRD : 0;
    const int nside = nrd + (iss ? C::PPW : 0);
#pragma unroll
    for (int nb = 0; nb < C::NB; ++nb)
#pragma unroll
      for (int mb = 0; mb < C::MB; ++mb) {
        const int idx = nb * C::MB + mb;
        if (SCALE) {
          t4[mb][nb] = __builtin_amdgcn_mfma_f32_16x16x32_bf16(cat8(bl[set][nb][0], bl[set][nb][1]), af[set][mb], (f32x4){0, 0, 0, 0}, 0, 0, 0);
        } else {
          acc[mb][nb] = __builtin_amdgcn_mfma_f32_16x16x32_bf16(cat8(bl[set][nb][0], bl[set][nb][1]), af[set][mb], acc[mb][nb], 0, 0, 0);
        }
        __builtin_amdgcn_sched_barrier(0);
#pragma unroll
        for (int e = 0; e < PER; ++e) {
          const int k = idx * PER + e;
          if (k < nrd) read1(rstage, rks, set ^ 1, k);
          else if (k < nside) issue_piece(dstage, k - nrd);
        }
        __builtin_amdgcn_sched_barrier(0);
      }
    if (SCALE) {
#pragma unroll
      for (int nb = 0; nb < C::NB; ++nb)
#pragma unroll
        for (int mb = 0; mb < C::MB; ++mb) {
#pragma unroll
          for (int r = 0; r < 4; ++r) acc[mb][nb][r] = fmaf(t4[mb][nb][r], scl[mb], acc[mb][nb][r]);
          asm volatile("" : "+v"(acc[mb][nb]));
        }
    }
  };
  const bool grpA = wid < C::NW / 2;

  waitv<C::PPW*(NSTAGE - 1)>();
  raw_barrier();
#pragma unroll
  for (int k = 0; k < NRD; ++k) read1(0, 0, 0, k);
  auto run = [&](auto GA) {
    constexpr bool ga = decltype(GA)::value;
#pragma unroll
    for (int kt = 0; kt < KT; ++kt) {
      const int cs = kt % NSTAGE, ns = (kt + 1) % NSTAGE, ps = (kt + NSTAGE - 1) % NSTAGE;
      __builtin_amdgcn_sched_barrier(0);
      phase(0, true, cs, 1, ga && kt >= 1 && kt - 1 + NSTAGE < KT, ps, 2 * kt);
      if (kt + 1 < KT) {
        if (kt + NSTAGE - 1 < KT) waitv<C::PPW*(NSTAGE - 2)>(); else waitv<0>();
        raw_barrier();
        __builtin_amdgcn_sched_barrier(0);
        phase(1, true, ns, 0, !ga && kt + NSTAGE < KT, cs, 2 * kt + 1);
      } else {
        phase(1, false, 0, 0, false, 0, 2 * kt + 1);
      }
    }
    __builtin_amdgcn_sched_barrier(0);
  };
  if (grpA) run(std::integral_constant<bool, true>{}); else run(std::integral_constant<bool, false>{});
  raw_barrier();
}

__global__ __launch_bounds__(256) void k_init(const u16* __restrict__ x, float2* __restrict__ stats,
                                              const u16* __restrict__ wA, const u16* __restrict__ wB,
                                              float* __restrict__ csA, float* __restrict__ csB) {
  const int tid = threadIdx.x;
  if (blockIdx.x < S_ / 4) {
    int row = blockIdx.x * 4 + (tid >> 6), lane = tid & 63;
    const bf16x8* px = (const bf16x8*)(x + (long)row * D_ + lane * 16);
    bf16x8 v0 = px[0], v1 = px[1];
    float f[16];
#pragma unroll
    for (int j = 0; j < 8; ++j) { f[j] = bf2f((u16)v0[j]); f[8 + j] = bf2f((u16)v1[j]); }
    float s = 0;
#pragma unroll
    for (int j = 0; j < 16; ++j) s += f[j];
    s += __shfl_xor(s, 1);
    float mu = s * (1.f / 32.f), m2 = 0;
#pragma unroll
    for (int j = 0; j < 16; ++j) { float d = f[j] - mu; m2 += d * d; }
    m2 += __shfl_xor(m2, 1);
    if ((lane & 1) == 0) stats[row * NPART + (lane >> 1)] = make_float2(s, m2);
  } else {
    int b = blockIdx.x - S_ / 4;
    int wsel = b / (12 * CSP); b %= 12 * CSP;
    int cb = b / CSP, rs = b % CSP;
    const u16* w = wsel ? wB : wA;
    float* cs = wsel ? csB : csA;
    __shared__ float red[8][256];
    int cg = tid & 31, rg = tid >> 5;
    float a[8] = {0, 0, 0, 0, 0, 0, 0, 0};
    bf16x8 v[16];
#pragma unroll
    for (int i = 0; i < 16; ++i) v[i] = *(const bf16x8*)(w + (long)(rs * 128 + rg + i * 8) * N3 + cb * 256 + cg * 8);
#pragma unroll
    for (int i = 0; i < 16; ++i)
#pragma unroll
      for (int j = 0; j < 8; ++j) a[j] += bf2f((u16)v[i][j]);
#pragma unroll
    for (int j = 0; j < 8; ++j) red[rg][cg * 8 + j] = a[j];
    __syncthreads();
    float t = 0;
#pragma unroll
    for (int i = 0; i < 8; ++i) t += red[i][tid];
    cs[rs * N3 + cb * 256 + tid] = t;
  }
}

using G1C = GemmCfg<128, 3, 2, 4, 3>;
constexpr int G1_TP = 200;

template <int MODE>
__global__ __launch_bounds__(512, 2) void k_qkv(const u16* __restrict__ x, const u16* __restrict__ w,
                                                const float2* __restrict__ stats, const float* __restrict__ csp,
                                                u16* __restrict__ out  , float* __restrict__ kvp,
                                                float* __restrict__ zp) {
  __shared__ __attribute__((aligned(16))) char smem[G1C::LDS_BYTES + 2048];
  float* s_mean = (float*)(smem + G1C::LDS_BYTES);
  float* s_rstd = s_mean + 128;
  float* s_cs = s_rstd + 128;
  const int tid = threadIdx.x, lane = tid & 63;
  int bid = blockIdx.x;
  int xcd = bid & 7, idx = bid >> 3;
  int mblk = (xcd & 1) * 8 + (idx & 7), nblk = (xcd >> 1) * 4 + (idx >> 3);
  const int m0 = mblk * 128;

  f32x4 acc[G1C::MB][G1C::NB];
#pragma unroll
  for (int i = 0; i < G1C::MB; ++i)
#pragma unroll
    for (int j = 0; j < G1C::NB; ++j) acc[i][j] = (f32x4){0, 0, 0, 0};

  auto pre = [&]() {
  {
    const int row = tid >> 2, pq_ = tid & 3;
    const f32x4* sp = (const f32x4*)(stats + (long)(m0 + row) * NPART + pq_ * 8);
    f32x4 pr[4];
#pragma unroll
    for (int i = 0; i < 4; ++i) pr[i] = sp[i];
    float s = 0;
#pragma unroll
    for (int i = 0; i < 4; ++i) s += pr[i][0] + pr[i][2];
    s += __shfl_xor(s, 1); s += __shfl_xor(s, 2);
    float mean = s * (1.f / D_), m2 = 0;
#pragma unroll
    for (int i = 0; i < 4; ++i) {
      float d0 = pr[i][0] * (1.f / 32.f) - mean, d1 = pr[i][2] * (1.f / 32.f) - mean;
      m2 += pr[i][1] + pr[i][3] + 32.f * (d0 * d0 + d1 * d1);
    }
    m2 += __shfl_xor(m2, 1); m2 += __shfl_xor(m2, 2);
    if (pq_ == 0) { s_mean[row] = mean; s_rstd[row] = rsqrtf(m2 * (1.f / D_) + 1e-5f); }
  }
  if (tid < 192) {
    int c = tid;
    int col = (c >> 6) * D_ + nblk * 64 + (c & 63);
    float t = 0;
#pragma unroll
    for (int i = 0; i < CSP; ++i) t += csp[i * N3 + col];
    s_cs[c] = t;
  }

  };
  gemm_mainloop<128, 3, 2, 4, 3, false>(pre, nullptr, x, D_, w, N3, m0, nblk * 64, D_, smem, acc);

  const int wid = tid >> 6, wm = wid / 4, wn = wid % 4;
  u16* T = (u16*)smem;
  const int l15 = lane & 15, g = lane >> 4;
#pragma unroll
  for (int mb = 0; mb < G1C::MB; ++mb) {
    const int row = wm * G1C::WTM + mb * 16 + l15;
    const float mean = s_mean[row], rstd = s_rstd[row];
#pragma unroll
    for (int nb = 0; nb < G1C::NB; ++nb) {
      const int col = wn * G1C::WTN + nb * 16 + g * 4;
      const f32x4 csv = *(const f32x4*)(s_cs + col);
      const int seg = col >> 6;
      float y[4];
#pragma unroll
      for (int r = 0; r < 4; ++r) {
        y[r] = (acc[mb][nb][r] - mean * csv[r]) * rstd;
        if (MODE == 0 && seg < 2) y[r] = phi_f(bfr(y[r]));
      }
      u32x2 w2;
      w2[0] = pack2(y[0], y[1]); w2[1] = pack2(y[2], y[3]);
      *(u32x2*)(T + row * G1_TP + col) = w2;
    }
  }
  __syncthreads();
  if (MODE == 1) {
#pragma unroll
    for (int i = 0; i < 6; ++i) {
      const int row = tid >> 2, part = tid & 3;
      u32x4 v = *(const u32x4*)(T + row * G1_TP + i * 32 + part * 8);
      *(u32x4*)(out + ((long)((i >> 1) * H_ + nblk * 2 + (i & 1)) * S_ + m0 + row) * DH + part * 8) = v;
    }
  } else {
#pragma unroll
    for (int i = 0; i < 2; ++i) {
      int ch = tid + i * 512;
      int row = ch >> 3, c = ch & 7;
      u32x4 v = *(const u32x4*)(T + row * G1_TP + c * 8);
      *(u32x4*)(out + (long)(m0 + row) * D_ + nblk * 64 + c * 8) = v;
    }
    int hh = wid >> 2, dt = (wid >> 1) & 1, et = wid & 1;
    int q = (lane & 15) >> 2, p = lane & 3;
    f32x4 kv = {0, 0, 0, 0}, zz = {0, 0, 0, 0};
    bf16x8 ones;
#pragma unroll
    for (int j = 0; j < 8; ++j) ones[j] = (short)0x3F80;
#pragma unroll
    for (int ks = 0; ks < 4; ++ks) {
      int r0 = ks * 32 + 8 * g + q;
      const char* pa = (const char*)(T + r0 * G1_TP + 64 + hh * 32 + dt * 16 + 4 * p);
      const char* pb = (const char*)(T + r0 * G1_TP + 128 + hh * 32 + et * 16 + 4 * p);
      bf16x8 a = cat8(tr_read(pa), tr_read(pa + 4 * G1_TP * 2));
      bf16x8 b = cat8(tr_read(pb), tr_read(pb + 4 * G1_TP * 2));
      kv = __builtin_amdgcn_mfma_f32_16x16x32_bf16(a, b, kv, 0, 0, 0);
      zz = __builtin_amdgcn_mfma_f32_16x16x32_bf16(a, ones, zz, 0, 0, 0);
    }
    int head = nblk * 2 + hh;
    float* kp = kvp + ((long)(mblk * H_ + head) * 32) * 32;
#pragma unroll
    for (int r = 0; r < 4; ++r) {
      int d = dt * 16 + g * 4 + r;
      kp[d * 32 + et * 16 + l15] = kv[r];
      if (et == 0 && l15 == 0) zp[mblk * D_ + head * 32 + d] = zz[r];
    }
  }
}

__global__ __launch_bounds__(256) void k_lin_mid(const float* __restrict__ kvp, const float* __restrict__ zp,
                                                 const u16* __restrict__ wo, u16* __restrict__ M,
                                                 u16* __restrict__ pq  ) {
  constexpr int WP = 264;
  constexpr int KP = 40;
  __shared__ __attribute__((aligned(16))) u16 sw[32 * WP];
  __shared__ __attribute__((aligned(16))) u16 skv[32 * KP];
  const int tid = threadIdx.x, lane = tid & 63, wid = tid >> 6;
  if (blockIdx.x >= 128) {
    const int r0 = (blockIdx.x - 128) * 8 + wid * 2;
    float* zs = (float*)sw;
    f32x4 zpr[16];
#pragma unroll
    for (int mb = 0; mb < 16; ++mb) zpr[mb] = *(const f32x4*)(zp + mb * D_ + tid * 4);
    bf16x8 v[2][2];
#pragma unroll
    for (int r = 0; r < 2; ++r) {
      const u16* pp = pq + (long)(r0 + r) * D_ + lane * 16;
      v[r][0] = *(const bf16x8*)(pp);
      v[r][1] = *(const bf16x8*)(pp + 8);
    }
    f32x4 zs4 = {0, 0, 0, 0};
#pragma unroll
    for (int mb = 0; mb < 16; ++mb) zs4 += zpr[mb];
    *(f32x4*)(zs + tid * 4) = zs4;
    __syncthreads();
    f32x4 z4[4];
#pragma unroll
    for (int i = 0; i < 4; ++i) z4[i] = *(const f32x4*)(zs + lane * 16 + i * 4);
#pragma unroll
    for (int r = 0; r < 2; ++r) {
      float den = 0;
#pragma unroll
      for (int i = 0; i < 2; ++i)
#pragma unroll
        for (int j = 0; j < 4; ++j)
          den += bf2f((u16)v[r][i][j]) * z4[2 * i][j] + bf2f((u16)v[r][i][4 + j]) * z4[2 * i + 1][j];
      den += __shfl_xor(den, 1);
      const float iv = 1.f / (den + 1e-6f);
      u16* pp = pq + (long)(r0 + r) * D_ + lane * 16;
#pragma unroll
      for (int i = 0; i < 2; ++i) {
        u32x4 o;
#pragma unroll
        for (int j = 0; j < 4; ++j) o[j] = pack2(bf2f((u16)v[r][i][2 * j]) * iv, bf2f((u16)v[r][i][2 * j + 1]) * iv);
        *(u32x4*)(pp + i * 8) = o;
      }
    }
    return;
  }
  const int h = blockIdx.x >> 2, nq = blockIdx.x & 3;
  f32x4 pr[16];
#pragma unroll
  for (int mb = 0; mb < 16; ++mb) pr[mb] = *(const f32x4*)(kvp + ((long)(mb * H_ + h) * 1024) + tid * 4);
  u32x4 wv[4];
#pragma unroll
  for (int i = 0; i < 4; ++i)
    wv[i] = *(const u32x4*)(wo + (long)(h * 32 + (tid >> 5) + i * 8) * D_ + nq * 256 + (tid & 31) * 8);
  f32x4 s4 = {0, 0, 0, 0};
#pragma unroll
  for (int mb = 0; mb < 16; ++mb) s4 += pr[mb];
  {
    u32x2 w2;
    w2[0] = pack2(s4[0], s4[1]); w2[1] = pack2(s4[2], s4[3]);
    *(u32x2*)(skv + (tid >> 3) * KP + (tid & 7) * 4) = w2;
  }
#pragma unroll
  for (int i = 0; i < 4; ++i) *(u32x4*)(sw + ((tid >> 5) + i * 8) * WP + (tid & 31) * 8) = wv[i];
  __syncthreads();
  const int l15 = lane & 15, g = lane >> 4, q = (lane & 15) >> 2, p = lane & 3;
  bf16x8 kf[2];
#pragma unroll
  for (int db = 0; db < 2; ++db) kf[db] = *(const bf16x8*)(skv + (db * 16 + l15) * KP + g * 8);
#pragma unroll
  for (int nb = 0; nb < 4; ++nb) {
    const int n0 = wid * 64 + nb * 16;
    const char* pa = (const char*)(sw + (8 * g + q) * WP + n0 + 4 * p);
    bf16x8 wf = cat8(tr_read(pa), tr_read(pa + 4 * WP * 2));
#pragma unroll
    for (int db = 0; db < 2; ++db) {
      f32x4 d4 = __builtin_amdgcn_mfma_f32_16x16x32_bf16(wf, kf[db], (f32x4){0, 0, 0, 0}, 0, 0, 0);
      u32x2 w2;
      w2[0] = pack2(d4[0], d4[1]); w2[1] = pack2(d4[2], d4[3]);
      *(u32x2*)(M + (long)(h * 32 + db * 16 + l15) * D_ + nq * 256 + n0 + 4 * g) = w2;
    }
  }
}

DEV int crow(int r, int hi) { return (r & 3) + 8 * (r >> 2) + 4 * hi; }

__global__ __launch_bounds__(256, 2) void k_swa(const u16* __restrict__ qkv, u16* __restrict__ att) {
  __shared__ __attribute__((aligned(16))) char sK[384 * 64];
  __shared__ __attribute__((aligned(16))) char sV[384 * 64];
  const int tid = threadIdx.x, lane = tid & 63, wid = tid >> 6;
  const int h = blockIdx.x & 31, qb = blockIdx.x >> 5;
  const int q0 = qb * 128, kbase = q0 - 256;
#pragma unroll
  for (int i = 0; i < 6; ++i) {
    int ch = tid + i * 256;
    int r = ch >> 2, c = ch & 3;
    int key = kbase + r;
    u32x4 kv4 = {0, 0, 0, 0}, vv4 = {0, 0, 0, 0};
    if (key >= 0) {
      kv4 = *(const u32x4*)(qkv + ((long)(H_ + h) * S_ + key) * DH + c * 8);
      vv4 = *(const u32x4*)(qkv + ((long)(2 * H_ + h) * S_ + key) * DH + c * 8);
    }
    *(u32x4*)(sK + r * 64 + ((c ^ ((r >> 2) & 3)) << 4)) = kv4;
    *(u32x4*)(sV + r * 64 + (c << 4)) =vv4;
  }
  const int l31 = lane & 31, hi = lane >> 5;
  const int qw = q0 + wid * 32;
  const int qi = qw + l31;
  bf16x8 qf[2];
#pragma unroll
  for (int ks = 0; ks < 2; ++ks) qf[ks] = *(const bf16x8*)(qkv + ((long)h * S_ + qi) * DH + ks * 16 + hi * 8);
  __syncthreads();

  typedef __attribute__((ext_vector_type(2))) float f32x2;
  const int kt0 = (qw >= 256) ? 0 : (256 - qw) / 32;
  f32x16 sc[9];
  const float scale_l2 = 0.17677669529663687f * 1.4426950408889634f;
  float mx = -3.0e38f;
  const f32x16 zero16 = {0, 0, 0, 0, 0, 0, 0, 0, 0, 0, 0, 0, 0, 0, 0, 0};
#pragma unroll
  for (int kt = 0; kt < 9; ++kt) {
    f32x16 a16;
    if (kt >= kt0) {
      int r = wid * 32 + kt * 32 + l31;
      bf16x8 kf0 = *(const bf16x8*)(sK + r * 64 + ((hi ^ ((r >> 2) & 3)) << 4));
      bf16x8 kf1 = *(const bf16x8*)(sK + r * 64 + (((2 + hi) ^ ((r >> 2) & 3)) << 4));
      a16 = __builtin_amdgcn_mfma_f32_32x32x16_bf16(kf0, qf[0], zero16, 0, 0, 0);
      a16 = __builtin_amdgcn_mfma_f32_32x32x16_bf16(kf1, qf[1], a16, 0, 0, 0);
      if (kt == 0) {
#pragma unroll
        for (int r2 = 0; r2 < 16; ++r2) a16[r2] = (crow(r2, hi) > l31) ? a16[r2] : -3.0e38f;
      }
      if (kt == 8) {
#pragma unroll
        for (int r2 = 0; r2 < 16; ++r2) a16[r2] = (crow(r2, hi) <= l31) ? a16[r2] : -3.0e38f;
      }
#pragma unroll
      for (int r2 = 0; r2 < 16; r2 += 2) mx = fmaxf(fmaxf(mx, a16[r2]), a16[r2 + 1]);
    } else {
#pragma unroll
      for (int i = 0; i < 16; ++i) a16[i] = -3.0e38f;
    }
    sc[kt] = a16;
  }
  mx = fmaxf(mx, __shfl_xor(mx, 32));
  const float mxs = -mx * scale_l2;
  const f32x2 sc2 = {scale_l2, scale_l2}, mx2 = {mxs, mxs};
  f32x2 sum2 = {0, 0};
#pragma unroll
  for (int kt = 0; kt < 9; ++kt)
#pragma unroll
    for (int r = 0; r < 16; r += 2) {
      f32x2 t = {sc[kt][r], sc[kt][r + 1]};
      t = t * sc2 + mx2;
      f32x2 e = {__builtin_amdgcn_exp2f(t[0]), __builtin_amdgcn_exp2f(t[1])};
      sc[kt][r] = e[0]; sc[kt][r + 1] = e[1];
      sum2 += e;
    }
  float sum = sum2[0] + sum2[1];
  sum += __shfl_xor(sum, 32);
  f32x16 o = zero16;
  const int q4 = (lane & 15) >> 2, p4 = lane & 3, blk = (lane >> 4) & 1;
#pragma unroll
  for (int kt = 0; kt < 9; ++kt) {
#pragma unroll
    for (int s = 0; s < 2; ++s) {
      u32x4 pk;
#pragma unroll
      for (int j = 0; j < 4; ++j) pk[j] = pack2(sc[kt][8 * s + 2 * j], sc[kt][8 * s + 2 * j + 1]);
      bf16x8 pf = __builtin_bit_cast(bf16x8, pk);
      int r0 = wid * 32 + kt * 32 + 16 * s + 4 * hi + q4;
      const char* vp = sV + r0 * 64 + (16 * blk + 4 * p4) * 2;
      bf16x8 vf = cat8(tr_read(vp), tr_read(vp + 8 * 64));
      o = __builtin_amdgcn_mfma_f32_32x32x16_bf16(vf, pf, o, 0, 0, 0);
    }
  }
  float inv = 1.f / sum;
  u16* op = att + (long)qi * D_ + h * 32;
#pragma unroll
  for (int gq = 0; gq < 4; ++gq) {
    u32x2 w2;
    w2[0] = pack2(o[4 * gq + 0] * inv, o[4 * gq + 1] * inv);
    w2[1] = pack2(o[4 * gq + 2] * inv, o[4 * gq + 3] * inv);
    *(u32x2*)(op + 8 * gq + 4 * hi) = w2;
  }
}

using G2C = GemmCfg<64, 2, 2, 4, 4>;

__global__ __launch_bounds__(512, 2) void k_out(const u16* __restrict__ a, const u16* __restrict__ w,
                                                const u16* __restrict__ xold, u16* __restrict__ xnew,
                                                float2* __restrict__ stats) {
  __shared__ __attribute__((aligned(16))) char smem[G2C::LDS_BYTES];
  const int tid = threadIdx.x, lane = tid & 63;
  int bid = blockIdx.x;
  int xcd = bid & 7, idx = bid >> 3;
  int mblk = xcd * 4 + (idx & 3), nblk = idx >> 2;
  const int m0 = mblk * 64;
  const int wid = tid >> 6, wm = wid / 4, wn = wid % 4;
  const int l15 = lane & 15, g = lane >> 4;
  f32x4 acc[G2C::MB][G2C::NB];
#pragma unroll
  for (int i = 0; i < G2C::MB; ++i)
#pragma unroll
    for (int j = 0; j < G2C::NB; ++j) acc[i][j] = (f32x4){0, 0, 0, 0};
  u32x2 xv[G2C::MB][G2C::NB];
  auto pre = [&]() {
#pragma unroll
    for (int mb = 0; mb < G2C::MB; ++mb)
#pragma unroll
      for (int nb = 0; nb < G2C::NB; ++nb)
        xv[mb][nb] = *(const u32x2*)(xold + (long)(m0 + wm * 32 + mb * 16 + l15) * D_ + nblk * 128 + wn * 32 + nb * 16 + g * 4);
  };
  gemm_mainloop<64, 2, 2, 4, 4, false>(pre, nullptr, a, D_, w, D_, m0, nblk * 128, 64, smem, acc);

#pragma unroll
  for (int mb = 0; mb < G2C::MB; ++mb) {
    const int row = m0 + wm * 32 + mb * 16 + l15;
    float v[8];
    float s = 0;
#pragma unroll
    for (int nb = 0; nb < G2C::NB; ++nb) {
      float f[4];
      f[0] = __uint_as_float(xv[mb][nb][0] << 16) + acc[mb][nb][0];
      f[1] = __uint_as_float(xv[mb][nb][0] & 0xffff0000u) + acc[mb][nb][1];
      f[2] = __uint_as_float(xv[mb][nb][1] << 16) + acc[mb][nb][2];
      f[3] = __uint_as_float(xv[mb][nb][1] & 0xffff0000u) + acc[mb][nb][3];
      u32x2 o;
      o[0] = pack2(f[0], f[1]); o[1] = pack2(f[2], f[3]);
      *(u32x2*)(xnew + (long)row * D_ + nblk * 128 + wn * 32 + nb * 16 + g * 4) = o;
      v[nb * 4 + 0] = __uint_as_float(o[0] << 16); v[nb * 4 + 1] = __uint_as_float(o[0] & 0xffff0000u);
      v[nb * 4 + 2] = __uint_as_float(o[1] << 16); v[nb * 4 + 3] = __uint_as_float(o[1] & 0xffff0000u);
    }
#pragma unroll
    for (int j = 0; j < 8; ++j) s += v[j];
    s += __shfl_xor(s, 16); s += __shfl_xor(s, 32);
    const float mu = s * (1.f / 32.f);
    float m2 = 0;
#pragma unroll
    for (int j = 0; j < 8; ++j) { float d = v[j] - mu; m2 += d * d; }
    m2 += __shfl_xor(m2, 16); m2 += __shfl_xor(m2, 32);
    if (g == 0) stats[(long)row * NPART + nblk * 4 + wn] = make_float2(s, m2);
  }
}

extern "C" void kernel_launch(void* const* d_in, const int* in_sizes, int n_in, void* d_out, int out_size,
                              void* d_ws, size_t ws_size, hipStream_t stream) {
  const u16* x_in = (const u16*)d_in[0];
  const u16* A_wqkv = (const u16*)d_in[1];
  const u16* A_wo = (const u16*)d_in[2];
  const u16* B_wqkv = (const u16*)d_in[4];
  const u16* B_wo = (const u16*)d_in[5];
  u16* out = (u16*)d_out;
  char* ws = (char*)d_ws;
  size_t off = 0;
  auto take = [&](size_t bytes) { char* p = ws + off; off += (bytes + 255) & ~(size_t)255; return p; };
  u16* xb0 = (u16*)take((size_t)S_ * D_ * 2);
  u16* xb1 = (u16*)take((size_t)S_ * D_ * 2);
  u16* qkv = (u16*)take((size_t)S_ * N3 * 2);
  u16* att = (u16*)take((size_t)S_ * D_ * 2);
  u16* Mm = (u16*)take((size_t)D_ * D_ * 2);
  float* kvp = (float*)take((size_t)16 * H_ * 1024 * 4);
  float* zp = (float*)take((size_t)16 * D_ * 4);
  float2* stats = (float2*)take((size_t)S_ * NPART * 8);
  float* csA = (float*)take((size_t)CSP * N3 * 4);
  float* csB = (float*)take((size_t)CSP * N3 * 4);
  float* zsum = (float*)take((size_t)S_ * 32 * 4);

  k_init<<<S_ / 4 + 2 * 12 * CSP, 256, 0, stream>>>(x_in, stats, A_wqkv, B_wqkv, csA, csB);
  k_qkv<0><<<256, 512, 0, stream>>>(x_in, A_wqkv, stats, csA, att, kvp, zp);
  k_lin_mid<<<384, 256, 0, stream>>>(kvp, zp, A_wo, Mm, att);
  k_out<<<256, 512, 0, stream>>>(att, Mm, x_in, xb0, stats);
  k_qkv<1><<<256, 512, 0, stream>>>(xb0, B_wqkv, stats, csB, qkv, kvp, zp);
  k_swa<<<512, 256, 0, stream>>>(qkv, att);
  k_out<<<256, 512, 0, stream>>>(att, B_wo, xb0, xb1, stats);
  k_qkv<0><<<256, 512, 0, stream>>>(xb1, A_wqkv, stats, csA, att, kvp, zp);
  k_lin_mid<<<384, 256, 0, stream>>>(kvp, zp, A_wo, Mm, att);
  k_out<<<256, 512, 0, stream>>>(att, Mm, xb1, xb0, stats);
  k_qkv<1><<<256, 512, 0, stream>>>(xb0, B_wqkv, stats, csB, qkv, kvp, zp);
  k_swa<<<512, 256, 0, stream>>>(qkv, att);
  k_out<<<256, 512, 0, stream>>>(att, B_wo, xb0, out, stats);
}
```

```cpp
#define ST_SC1
#include <hip/hip_runtime.h>
#include <hip/hip_bf16.h>
#include <stdint.h>
#include <type_traits>

constexpr int S_ = 2048, D_ = 1024, H_ = 32, DH = 32, WIN = 256;
constexpr int N3 = 3 * D_;
constexpr int NPART = 8;
constexpr int CSP = 8;

typedef __attribute__((ext_vector_type(8))) short bf16x8;
typedef __attribute__((ext_vector_type(4))) short s16x4;
typedef __attribute__((ext_vector_type(4))) float f32x4;
typedef __attribute__((ext_vector_type(16))) float f32x16;
typedef __attribute__((ext_vector_type(4))) unsigned u32x4;
typedef __attribute__((ext_vector_type(2))) unsigned u32x2;
typedef __attribute__((address_space(3))) s16x4* lds_s16x4_p;
typedef unsigned short u16;

#define DEV __device__ __forceinline__

DEV float bf2f(u16 v) { return __uint_as_float(((unsigned)v) << 16); }
typedef __attribute__((ext_vector_type(2))) float f32x2_t;
typedef __attribute__((ext_vector_type(2))) __bf16 bf16x2_t;
DEV unsigned pack2(float lo, float hi) {
  f32x2_t v = {lo, hi};
  bf16x2_t b = __builtin_convertvector(v, bf16x2_t);
  return __builtin_bit_cast(unsigned, b);
}
DEV u16 f2bf(float f) { return (u16)(pack2(f, f) & 0xffffu); }
DEV float bfr(float f) { return __uint_as_float(pack2(f, f) << 16); }
DEV float phi_f(float t) { return (t > 0.f ? t + 1.f : __expf(t)) + 1e-6f; }

DEV void st16(void* p, u32x4 v) {
  asm volatile("global_store_dwordx4 %0, %1, off sc1\n\ts_nop 1" ::"v"(p), "v"(v) : "memory");
}
DEV s16x4 tr_read(const char* p) {
  return __builtin_amdgcn_ds_read_tr16_b64_v4i16((lds_s16x4_p)(p));
}
DEV bf16x8 cat8(s16x4 a, s16x4 b) { return __builtin_shufflevector(a, b, 0, 1, 2, 3, 4, 5, 6, 7); }

#define WAITV(n) asm volatile("s_waitcnt vmcnt(" #n ")" ::: "memory")
template <int N> DEV void waitv() { asm volatile("s_waitcnt vmcnt(%0)" ::"n"(N) : "memory"); }
DEV void wait_lgkm0() { __builtin_amdgcn_s_waitcnt(0xC07F); }
DEV void raw_barrier() {
  asm volatile("" ::: "memory");
  wait_lgkm0();
  __builtin_amdgcn_s_barrier();
  asm volatile("" ::: "memory");
}

template <int BM, int NSEG, int WGM, int WGN, int NSTAGE>
struct GemmCfg {
  static constexpr int NW = WGM * WGN;
  static constexpr int NT_ = NW * 64;
  static constexpr int BN = NSEG * 64;
  static constexpr int WTM = BM / WGM, WTN = BN / WGN;
  static constexpr int MB = WTM / 16, NB = WTN / 16;
  static constexpr int APIECES = BM / 8, BPIECES = NSEG * 8;
  static constexpr int PPW = (APIECES + BPIECES) / NW;
  static constexpr int STAGE_BYTES = (APIECES + BPIECES) * 1024;
  static constexpr int LDS_BYTES = STAGE_BYTES * NSTAGE;
  static_assert((APIECES + BPIECES) % NW == 0, "pieces");
};

template <int BM, int NSEG, int WGM, int WGN, int NSTAGE, bool SCALE, typename PRE>
DEV void gemm_mainloop(PRE pre, const float* sinv  , const u16* __restrict__ A, int lda, const u16* __restrict__ B, int ldb,
                       int m0, int colbase0, int segstride, char* smem,
                       f32x4 (&acc)[GemmCfg<BM, NSEG, WGM, WGN, NSTAGE>::MB][GemmCfg<BM, NSEG, WGM, WGN, NSTAGE>::NB]) {
  using C = GemmCfg<BM, NSEG, WGM, WGN, NSTAGE>;
  constexpr int KT = D_ / 64;
  const int tid = threadIdx.x, lane = tid & 63;
  const int wid = __builtin_amdgcn_readfirstlane(tid >> 6);
  const int wm = wid / WGN, wn = wid % WGN;

  const u16* gp[C::PPW];
  int gstep[C::PPW];

#pragma unroll
  for (int i = 0; i < C::PPW; ++i) {
    int p = wid * C::PPW + i;
    if (p < C::APIECES) {
      int row = p * 8 + (lane >> 3), slot = lane & 7;
      int chunk = slot ^ ((row >> 1) & 7);
      gstep[i] = 64;
      gp[i] = A + (long)(m0 + row) * lda + chunk * 8;
    } else {
      int pb = p - C::APIECES;
      int seg = pb >> 3, k = (pb & 7) * 8 + (lane >> 3), slot = lane & 7;
      int f = ((k >> 1) & 1) | (((k >> 3) & 1) << 1);
      int chunk = slot ^ (f << 1);
      int cb = colbase0 + seg * segstride;
      gstep[i] = 64 * ldb;
      gp[i] = B + (long)k * ldb + cb + chunk * 8;
    }
  }
  auto issue_piece = [&](int stage, int i) {
    char* base = smem + stage * C::STAGE_BYTES + wid * C::PPW * 1024;
    __builtin_amdgcn_global_load_lds((const unsigned*)gp[i], (unsigned*)(base + i * 1024), 16, 0, 0);
    gp[i] += gstep[i];
  };

  const int l15 = lane & 15, g = lane >> 4;
  const int a_off = (wm * C::WTM + l15) * 128;
  const int a_sw = (l15 >> 1);
  const int q = (lane & 15) >> 2, p = lane & 3;
  const int fB = ((q >> 1) & 1) | ((g & 1) << 1);
  const int b_base = C::APIECES * 1024 + (8 * g + q) * 128 + (p & 1) * 8;

#pragma unroll
  for (int s = 0; s < NSTAGE; ++s)
#pragma unroll
    for (int i = 0; i < C::PPW; ++i) issue_piece(s, i);
  pre();

  bf16x8 af[2][C::MB];
  s16x4 bl[2][C::NB][2];
  constexpr int NM = C::NB * C::MB;
  constexpr int NRD = C::NB * 2 + C::MB;
  auto read1 = [&](int stage, int ks, int set, int k) {
    const char* sb = smem + stage * C::STAGE_BYTES;
    if (k < C::NB * 2) {
      int nb = k >> 1, hf = k & 1;
      int gnb = wn * C::NB + nb;
      int seg = gnb >> 2, c = gnb & 3;
      const char* bp = sb + b_base + seg * 8192 + ks * 4096 + ((((2 * c + (p >> 1)) ^ (fB << 1))) << 4);
      bl[set][nb][hf] = tr_read(bp + hf * 512);
    } else {
      int mb = k - C::NB * 2;
      af[set][mb] = *(const bf16x8*)(sb + a_off + mb * 2048 + (((ks * 4 + g) ^ a_sw) << 4));
    }
  };
  auto phase = [&](int set, bool rd, int rstage, int rks, bool iss, int dstage, int head) {
    float scl[C::MB];
    f32x4 t4[C::MB][C::NB];
    if (SCALE) {
      asm volatile("" ::: "memory");
#pragma unroll
      for (int mb = 0; mb < C::MB; ++mb) scl[mb] = sinv[(wm * C::WTM + mb * 16 + l15) * 33 + head];
    }
    constexpr int NSIDE_MAX = NRD + C::PPW;
    constexpr int PER = (NSIDE_MAX + NM - 1) / NM;
    const int nrd = rd ? NRD : 0;
    const int nside = nrd + (iss ? C::PPW : 0);
#pragma unroll
    for (int nb = 0; nb < C::NB; ++nb)
#pragma unroll
      for (int mb = 0; mb < C::MB; ++mb) {
        const int idx = nb * C::MB + mb;
        if (SCALE) {
          t4[mb][nb] = __builtin_amdgcn_mfma_f32_16x16x32_bf16(cat8(bl[set][nb][0], bl[set][nb][1]), af[set][mb], (f32x4){0, 0, 0, 0}, 0, 0, 0);
        } else {
          acc[mb][nb] = __builtin_amdgcn_mfma_f32_16x16x32_bf16(cat8(bl[set][nb][0], bl[set][nb][1]), af[set][mb], acc[mb][nb], 0, 0, 0);
        }
        __builtin_amdgcn_sched_barrier(0);
#pragma unroll
        for (int e = 0; e < PER; ++e) {
          const int k = idx * PER + e;
          if (k < nrd) read1(rstage, rks, set ^ 1, k);
          else if (k < nside) issue_piece(dstage, k - nrd);
        }
        __builtin_amdgcn_sched_barrier(0);
      }
    if (SCALE) {
#pragma unroll
      for (int nb = 0; nb < C::NB; ++nb)
#pragma unroll
        for (int mb = 0; mb < C::MB; ++mb) {
#pragma unroll
          for (int r = 0; r < 4; ++r) acc[mb][nb][r] = fmaf(t4[mb][nb][r], scl[mb], acc[mb][nb][r]);
          asm volatile("" : "+v"(acc[mb][nb]));
        }
    }
  };
  const bool grpA = wid < C::NW / 2;

  waitv<C::PPW*(NSTAGE - 1)>();
  raw_barrier();
#pragma unroll
  for (int k = 0; k < NRD; ++k) read1(0, 0, 0, k);
  auto run = [&](auto GA) {
    constexpr bool ga = decltype(GA)::value;
#pragma unroll
    for (int kt = 0; kt < KT; ++kt) {
      const int cs = kt % NSTAGE, ns = (kt + 1) % NSTAGE, ps = (kt + NSTAGE - 1) % NSTAGE;
      __builtin_amdgcn_sched_barrier(0);
      phase(0, true, cs, 1, ga && kt >= 1 && kt - 1 + NSTAGE < KT, ps, 2 * kt);
      if (kt + 1 < KT) {
        if (kt + NSTAGE - 1 < KT) waitv<C::PPW*(NSTAGE - 2)>(); else waitv<0>();
        raw_barrier();
        __builtin_amdgcn_sched_barrier(0);
        phase(1, true, ns, 0, !ga && kt + NSTAGE < KT, cs, 2 * kt + 1);
      } else {
        phase(1, false, 0, 0, false, 0, 2 * kt + 1);
      }
    }
    __builtin_amdgcn_sched_barrier(0);
  };
  if (grpA) run(std::integral_constant<bool, true>{}); else run(std::integral_constant<bool, false>{});
  raw_barrier();
}

__global__ __launch_bounds__(256) void k_init(const u16* __restrict__ x, float2* __restrict__ stats,
                                              const u16* __restrict__ wA, const u16* __restrict__ wB,
                                              float* __restrict__ csA, float* __restrict__ csB) {
  const int tid = threadIdx.x;
  if (blockIdx.x < S_ / 4) {
    int row = blockIdx.x * 4 + (tid >> 6), lane = tid & 63;
    const bf16x8* px = (const bf16x8*)(x + (long)row * D_ + lane * 16);
    bf16x8 v0 = px[0], v1 = px[1];
    float f[16];
#pragma unroll
    for (int j = 0; j < 8; ++j) { f[j] = bf2f((u16)v0[j]); f[8 + j] = bf2f((u16)v1[j]); }
    float s = 0;
#pragma unroll
    for (int j = 0; j < 16; ++j) s += f[j];
    s += __shfl_xor(s, 1); s += __shfl_xor(s, 2); s += __shfl_xor(s, 4);
    float mu = s * (1.f / 128.f), m2 = 0;
#pragma unroll
    for (int j = 0; j < 16; ++j) { float d = f[j] - mu; m2 += d * d; }
    m2 += __shfl_xor(m2, 1); m2 += __shfl_xor(m2, 2); m2 += __shfl_xor(m2, 4);
    if ((lane & 7) == 0) stats[row * NPART + (lane >> 3)] = make_float2(s, m2);
  } else {
    int b = blockIdx.x - S_ / 4;
    int wsel = b / (12 * CSP); b %= 12 * CSP;
    int cb = b / CSP, rs = b % CSP;
    const u16* w = wsel ? wB : wA;
    float* cs = wsel ? csB : csA;
    __shared__ float red[8][256];
    int cg = tid & 31, rg = tid >> 5;
    float a[8] = {0, 0, 0, 0, 0, 0, 0, 0};
    bf16x8 v[16];
#pragma unroll
    for (int i = 0; i < 16; ++i) v[i] = *(const bf16x8*)(w + (long)(rs * 128 + rg + i * 8) * N3 + cb * 256 + cg * 8);
#pragma unroll
    for (int i = 0; i < 16; ++i)
#pragma unroll
      for (int j = 0; j < 8; ++j) a[j] += bf2f((u16)v[i][j]);
#pragma unroll
    for (int j = 0; j < 8; ++j) red[rg][cg * 8 + j] = a[j];
    __syncthreads();
    float t = 0;
#pragma unroll
    for (int i = 0; i < 8; ++i) t += red[i][tid];
    cs[rs * N3 + cb * 256 + tid] = t;
  }
}

using G1C = GemmCfg<128, 3, 2, 4, 3>;
constexpr int G1_TP = 200;

template <int MODE>
__global__ __launch_bounds__(512, 2) void k_qkv(const u16* __restrict__ x, const u16* __restrict__ w,
                                                const float2* __restrict__ stats, const float* __restrict__ csp,
                                                u16* __restrict__ out  , float* __restrict__ kvp,
                                                float* __restrict__ zp) {
  __shared__ __attribute__((aligned(16))) char smem[G1C::LDS_BYTES + 2048];
  float* s_mean = (float*)(smem + G1C::LDS_BYTES);
  float* s_rstd = s_mean + 128;
  float* s_cs = s_rstd + 128;
  const int tid = threadIdx.x, lane = tid & 63;
  int bid = blockIdx.x;
  int xcd = bid & 7, idx = bid >> 3;
  int mblk = (xcd & 1) * 8 + (idx & 7), nblk = (xcd >> 1) * 4 + (idx >> 3);
  const int m0 = mblk * 128;

  f32x4 acc[G1C::MB][G1C::NB];
#pragma unroll
  for (int i = 0; i < G1C::MB; ++i)
#pragma unroll
    for (int j = 0; j < G1C::NB; ++j) acc[i][j] = (f32x4){0, 0, 0, 0};

  auto pre = [&]() {
  if (tid < 128) {
    const float2* sp = stats + (long)(m0 + tid) * NPART;
    float2 pr[NPART];
#pragma unroll
    for (int i = 0; i < NPART; ++i) pr[i] = sp[i];
    float s = 0;
#pragma unroll
    for (int i = 0; i < NPART; ++i) s += pr[i].x;
    float mean = s * (1.f / D_), m2 = 0;
#pragma unroll
    for (int i = 0; i < NPART; ++i) { float d = pr[i].x * (1.f / 128.f) - mean; m2 += pr[i].y + 128.f * d * d; }
    s_mean[tid] = mean;
    s_rstd[tid] = rsqrtf(m2 * (1.f / D_) + 1e-5f);
  } else if (tid < 128 + 192) {
    int c = tid - 128;
    int col = (c >> 6) * D_ + nblk * 64 + (c & 63);
    float t = 0;
#pragma unroll
    for (int i = 0; i < CSP; ++i) t += csp[i * N3 + col];
    s_cs[c] = t;
  }

  };
  gemm_mainloop<128, 3, 2, 4, 3, false>(pre, nullptr, x, D_, w, N3, m0, nblk * 64, D_, smem, acc);

  const int wid = tid >> 6, wm = wid / 4, wn = wid % 4;
  u16* T = (u16*)smem;
  const int l15 = lane & 15, g = lane >> 4;
#pragma unroll
  for (int mb = 0; mb < G1C::MB; ++mb) {
    const int row = wm * G1C::WTM + mb * 16 + l15;
    const float mean = s_mean[row], rstd = s_rstd[row];
#pragma unroll
    for (int nb = 0; nb < G1C::NB; ++nb) {
      const int col = wn * G1C::WTN + nb * 16 + g * 4;
      const f32x4 csv = *(const f32x4*)(s_cs + col);
      const int seg = col >> 6;
      float y[4];
#pragma unroll
      for (int r = 0; r < 4; ++r) {
        y[r] = (acc[mb][nb][r] - mean * csv[r]) * rstd;
        if (MODE == 0 && seg < 2) y[r] = phi_f(bfr(y[r]));
      }
      u32x2 w2;
      w2[0] = pack2(y[0], y[1]); w2[1] = pack2(y[2], y[3]);
      *(u32x2*)(T + row * G1_TP + col) = w2;
    }
  }
  __syncthreads();
  if (MODE == 1) {
#pragma unroll
    for (int i = 0; i < 6; ++i) {
      const int row = tid >> 2, part = tid & 3;
      u32x4 v = *(const u32x4*)(T + row * G1_TP + i * 32 + part * 8);
      st16(out + ((long)((i >> 1) * H_ + nblk * 2 + (i & 1)) * S_ + m0 + row) * DH + part * 8, v);
    }
  } else {
#pragma unroll
    for (int i = 0; i < 2; ++i) {
      int ch = tid + i * 512;
      int row = ch >> 3, c = ch & 7;
      u32x4 v = *(const u32x4*)(T + row * G1_TP + c * 8);
      st16(out + (long)(m0 + row) * D_ + nblk * 64 + c * 8, v);
    }
    int hh = wid >> 2, dt = (wid >> 1) & 1, et = wid & 1;
    int q = (lane & 15) >> 2, p = lane & 3;
    f32x4 kv = {0, 0, 0, 0}, zz = {0, 0, 0, 0};
    bf16x8 ones;
#pragma unroll
    for (int j = 0; j < 8; ++j) ones[j] = (short)0x3F80;
#pragma unroll
    for (int ks = 0; ks < 4; ++ks) {
      int r0 = ks * 32 + 8 * g + q;
      const char* pa = (const char*)(T + r0 * G1_TP + 64 + hh * 32 + dt * 16 + 4 * p);
      const char* pb = (const char*)(T + r0 * G1_TP + 128 + hh * 32 + et * 16 + 4 * p);
      bf16x8 a = cat8(tr_read(pa), tr_read(pa + 4 * G1_TP * 2));
      bf16x8 b = cat8(tr_read(pb), tr_read(pb + 4 * G1_TP * 2));
      kv = __builtin_amdgcn_mfma_f32_16x16x32_bf16(a, b, kv, 0, 0, 0);
      zz = __builtin_amdgcn_mfma_f32_16x16x32_bf16(a, ones, zz, 0, 0, 0);
    }
    int head = nblk * 2 + hh;
    float* kp = kvp + ((long)(mblk * H_ + head) * 32) * 32;
#pragma unroll
    for (int r = 0; r < 4; ++r) {
      int d = dt * 16 + g * 4 + r;
      kp[d * 32 + et * 16 + l15] = kv[r];
      if (et == 0 && l15 == 0) zp[mblk * D_ + head * 32 + d] = zz[r];
    }
  }
}

__global__ __launch_bounds__(256) void k_lin_mid(const float* __restrict__ kvp, const float* __restrict__ zp,
                                                 const u16* __restrict__ wo, u16* __restrict__ M,
                                                 u16* __restrict__ pq  ) {
  constexpr int WP = 264;
  constexpr int KP = 40;
  __shared__ __attribute__((aligned(16))) u16 sw[32 * WP];
  __shared__ __attribute__((aligned(16))) u16 skv[32 * KP];
  const int tid = threadIdx.x, lane = tid & 63, wid = tid >> 6;
  if (blockIdx.x >= 128) {
    const int r0 = (blockIdx.x - 128) * 8 + wid * 2;
    float* zs = (float*)sw;
    f32x4 zpr[16];
#pragma unroll
    for (int mb = 0; mb < 16; ++mb) zpr[mb] = *(const f32x4*)(zp + mb * D_ + tid * 4);
    bf16x8 v[2][2];
#pragma unroll
    for (int r = 0; r < 2; ++r) {
      const u16* pp = pq + (long)(r0 + r) * D_ + lane * 16;
      v[r][0] = *(const bf16x8*)(pp);
      v[r][1] = *(const bf16x8*)(pp + 8);
    }
    f32x4 zs4 = {0, 0, 0, 0};
#pragma unroll
    for (int mb = 0; mb < 16; ++mb) zs4 += zpr[mb];
    *(f32x4*)(zs + tid * 4) = zs4;
    __syncthreads();
    f32x4 z4[4];
#pragma unroll
    for (int i = 0; i < 4; ++i) z4[i] = *(const f32x4*)(zs + lane * 16 + i * 4);
#pragma unroll
    for (int r = 0; r < 2; ++r) {
      float den = 0;
#pragma unroll
      for (int i = 0; i < 2; ++i)
#pragma unroll
        for (int j = 0; j < 4; ++j)
          den += bf2f((u16)v[r][i][j]) * z4[2 * i][j] + bf2f((u16)v[r][i][4 + j]) * z4[2 * i + 1][j];
      den += __shfl_xor(den, 1);
      const float iv = 1.f / (den + 1e-6f);
      u16* pp = pq + (long)(r0 + r) * D_ + lane * 16;
#pragma unroll
      for (int i = 0; i < 2; ++i) {
        u32x4 o;
#pragma unroll
        for (int j = 0; j < 4; ++j) o[j] = pack2(bf2f((u16)v[r][i][2 * j]) * iv, bf2f((u16)v[r][i][2 * j + 1]) * iv);
        *(u32x4*)(pp + i * 8) = o;
      }
    }
    return;
  }
  const int h = blockIdx.x >> 2, nq = blockIdx.x & 3;
  f32x4 pr[16];
#pragma unroll
  for (int mb = 0; mb < 16; ++mb) pr[mb] = *(const f32x4*)(kvp + ((long)(mb * H_ + h) * 1024) + tid * 4);
  u32x4 wv[4];
#pragma unroll
  for (int i = 0; i < 4; ++i)
    wv[i] = *(const u32x4*)(wo + (long)(h * 32 + (tid >> 5) + i * 8) * D_ + nq * 256 + (tid & 31) * 8);
  f32x4 s4 = {0, 0, 0, 0};
#pragma unroll
  for (int mb = 0; mb < 16; ++mb) s4 += pr[mb];
  {
    u32x2 w2;
    w2[0] = pack2(s4[0], s4[1]); w2[1] = pack2(s4[2], s4[3]);
    *(u32x2*)(skv + (tid >> 3) * KP + (tid & 7) * 4) = w2;
  }
#pragma unroll
  for (int i = 0; i < 4; ++i) *(u32x4*)(sw + ((tid >> 5) + i * 8) * WP + (tid & 31) * 8) = wv[i];
  __syncthreads();
  const int l15 = lane & 15, g = lane >> 4, q = (lane & 15) >> 2, p = lane & 3;
  bf16x8 kf[2];
#pragma unroll
  for (int db = 0; db < 2; ++db) kf[db] = *(const bf16x8*)(skv + (db * 16 + l15) * KP + g * 8);
#pragma unroll
  for (int nb = 0; nb < 4; ++nb) {
    const int n0 = wid * 64 + nb * 16;
    const char* pa = (const char*)(sw + (8 * g + q) * WP + n0 + 4 * p);
    bf16x8 wf = cat8(tr_read(pa), tr_read(pa + 4 * WP * 2));
#pragma unroll
    for (int db = 0; db < 2; ++db) {
      f32x4 d4 = __builtin_amdgcn_mfma_f32_16x16x32_bf16(wf, kf[db], (f32x4){0, 0, 0, 0}, 0, 0, 0);
      u32x2 w2;
      w2[0] = pack2(d4[0], d4[1]); w2[1] = pack2(d4[2], d4[3]);
      *(u32x2*)(M + (long)(h * 32 + db * 16 + l15) * D_ + nq * 256 + n0 + 4 * g) = w2;
    }
  }
}

DEV int crow(int r, int hi) { return (r & 3) + 8 * (r >> 2) + 4 * hi; }

__global__ __launch_bounds__(256, 2) void k_swa(const u16* __restrict__ qkv, u16* __restrict__ att) {
  __shared__ __attribute__((aligned(16))) char sK[384 * 64];
  __shared__ __attribute__((aligned(16))) char sV[384 * 64];
  const int tid = threadIdx.x, lane = tid & 63, wid = tid >> 6;
  const int h = blockIdx.x & 31, qb = blockIdx.x >> 5;
  const int q0 = qb * 128, kbase = q0 - 256;
#pragma unroll
  for (int i = 0; i < 6; ++i) {
    int ch = tid + i * 256;
    int r = ch >> 2, c = ch & 3;
    int key = kbase + r;
    u32x4 kv4 = {0, 0, 0, 0}, vv4 = {0, 0, 0, 0};
    if (key >= 0) {
      kv4 = *(const u32x4*)(qkv + ((long)(H_ + h) * S_ + key) * DH + c * 8);
      vv4 = *(const u32x4*)(qkv + ((long)(2 * H_ + h) * S_ + key) * DH + c * 8);
    }
    *(u32x4*)(sK + r * 64 + ((c ^ ((r >> 2) & 3)) << 4)) = kv4;
    *(u32x4*)(sV + r * 64 + (c << 4)) =vv4;
  }
  const int l31 = lane & 31, hi = lane >> 5;
  const int qw = q0 + wid * 32;
  const int qi = qw + l31;
  bf16x8 qf[2];
#pragma unroll
  for (int ks = 0; ks < 2; ++ks) qf[ks] = *(const bf16x8*)(qkv + ((long)h * S_ + qi) * DH + ks * 16 + hi * 8);
  __syncthreads();

  typedef __attribute__((ext_vector_type(2))) float f32x2;
  const int kt0 = (qw >= 256) ? 0 : (256 - qw) / 32;
  f32x16 sc[9];
  const float scale_l2 = 0.17677669529663687f * 1.4426950408889634f;
  float mx = -3.0e38f;
  const f32x16 zero16 = {0, 0, 0, 0, 0, 0, 0, 0, 0, 0, 0, 0, 0, 0, 0, 0};
#pragma unroll
  for (int kt = 0; kt < 9; ++kt) {
    f32x16 a16;
    if (kt >= kt0) {
      int r = wid * 32 + kt * 32 + l31;
      bf16x8 kf0 = *(const bf16x8*)(sK + r * 64 + ((hi ^ ((r >> 2) & 3)) << 4));
      bf16x8 kf1 = *(const bf16x8*)(sK + r * 64 + (((2 + hi) ^ ((r >> 2) & 3)) << 4));
      a16 = __builtin_amdgcn_mfma_f32_32x32x16_bf16(kf0, qf[0], zero16, 0, 0, 0);
      a16 = __builtin_amdgcn_mfma_f32_32x32x16_bf16(kf1, qf[1], a16, 0, 0, 0);
      if (kt == 0) {
#pragma unroll
        for (int r2 = 0; r2 < 16; ++r2) a16[r2] = (crow(r2, hi) > l31) ? a16[r2] : -3.0e38f;
      }
      if (kt == 8) {
#pragma unroll
        for (int r2 = 0; r2 < 16; ++r2) a16[r2] = (crow(r2, hi) <= l31) ? a16[r2] : -3.0e38f;
      }
#pragma unroll
      for (int r2 = 0; r2 < 16; r2 += 2) mx = fmaxf(fmaxf(mx, a16[r2]), a16[r2 + 1]);
    } else {
#pragma unroll
      for (int i = 0; i < 16; ++i) a16[i] = -3.0e38f;
    }
    sc[kt] = a16;
  }
  mx = fmaxf(mx, __shfl_xor(mx, 32));
  const float mxs = -mx * scale_l2;
  const f32x2 sc2 = {scale_l2, scale_l2}, mx2 = {mxs, mxs};
  f32x2 sum2 = {0, 0};
#pragma unroll
  for (int kt = 0; kt < 9; ++kt)
#pragma unroll
    for (int r = 0; r < 16; r += 2) {
      f32x2 t = {sc[kt][r], sc[kt][r + 1]};
      t = t * sc2 + mx2;
      f32x2 e = {__builtin_amdgcn_exp2f(t[0]), __builtin_amdgcn_exp2f(t[1])};
      sc[kt][r] = e[0]; sc[kt][r + 1] = e[1];
      sum2 += e;
    }
  float sum = sum2[0] + sum2[1];
  sum += __shfl_xor(sum, 32);
  f32x16 o = zero16;
  const int q4 = (lane & 15) >> 2, p4 = lane & 3, blk = (lane >> 4) & 1;
#pragma unroll
  for (int kt = 0; kt < 9; ++kt) {
#pragma unroll
    for (int s = 0; s < 2; ++s) {
      u32x4 pk;
#pragma unroll
      for (int j = 0; j < 4; ++j) pk[j] = pack2(sc[kt][8 * s + 2 * j], sc[kt][8 * s + 2 * j + 1]);
      bf16x8 pf = __builtin_bit_cast(bf16x8, pk);
      int r0 = wid * 32 + kt * 32 + 16 * s + 4 * hi + q4;
      const char* vp = sV + r0 * 64 + (16 * blk + 4 * p4) * 2;
      bf16x8 vf = cat8(tr_read(vp), tr_read(vp + 8 * 64));
      o = __builtin_amdgcn_mfma_f32_32x32x16_bf16(vf, pf, o, 0, 0, 0);
    }
  }
  float inv = 1.f / sum;
  u16* op = att + (long)qi * D_ + h * 32;
#pragma unroll
  for (int gq = 0; gq < 4; ++gq) {
    u32x2 w2;
    w2[0] = pack2(o[4 * gq + 0] * inv, o[4 * gq + 1] * inv);
    w2[1] = pack2(o[4 * gq + 2] * inv, o[4 * gq + 3] * inv);
    *(u32x2*)(op + 8 * gq + 4 * hi) = w2;
  }
}

using G2C = GemmCfg<64, 2, 2, 4, 4>;

template <bool LIN>
__global__ __launch_bounds__(512, 2) void k_out(const u16* __restrict__ a, const u16* __restrict__ w,
                                                const u16* __restrict__ xold, u16* __restrict__ xnew,
                                                float2* __restrict__ stats, const float* __restrict__ zsum) {
  __shared__ __attribute__((aligned(16))) char smem[G2C::LDS_BYTES + 64 * 33 * 4];
  float* sinv = (float*)(smem + G2C::LDS_BYTES);
  const int tid = threadIdx.x, lane = tid & 63;
  int bid = blockIdx.x;
  int xcd = bid & 7, idx = bid >> 3;
  int mblk = xcd * 4 + (idx & 3), nblk = idx >> 2;
  const int m0 = mblk * 64;
  f32x4 acc[G2C::MB][G2C::NB];
#pragma unroll
  for (int i = 0; i < G2C::MB; ++i)
#pragma unroll
    for (int j = 0; j < G2C::NB; ++j) acc[i][j] = (f32x4){0, 0, 0, 0};
  auto pre = [&]() {
    if (LIN) {
      f32x4 v4 = *(const f32x4*)(zsum + (long)m0 * 32 + tid * 4);
      float* dst = sinv + (tid >> 3) * 33 + (tid & 7) * 4;
#pragma unroll
      for (int j = 0; j < 4; ++j) dst[j] = v4[j];
    }
  };
  gemm_mainloop<64, 2, 2, 4, 4, LIN>(pre, sinv, a, D_, w, D_, m0, nblk * 128, 64, smem, acc);

  const int wid = tid >> 6, wm = wid / 4, wn = wid % 4;
  float* T = (float*)smem;
  constexpr int TP = 132;
  const int l15 = lane & 15, g = lane >> 4;
#pragma unroll
  for (int mb = 0; mb < G2C::MB; ++mb)
#pragma unroll
    for (int nb = 0; nb < G2C::NB; ++nb)
      *(f32x4*)(T + (wm * 32 + mb * 16 + l15) * TP + wn * 32 + nb * 16 + g * 4) = acc[mb][nb];
  __syncthreads();
  int row = tid >> 3, part = tid & 7;
  const u16* xo = xold + (long)(m0 + row) * D_ + nblk * 128 + part * 16;
  u16* xn = xnew + (long)(m0 + row) * D_ + nblk * 128 + part * 16;
  float v[16];
  float s = 0;
#pragma unroll
  for (int i = 0; i < 2; ++i) {
    bf16x8 xv = *(const bf16x8*)(xo + i * 8);
    u32x4 o;
#pragma unroll
    for (int j = 0; j < 4; ++j) {
      float f0 = bfr(bf2f((u16)xv[2 * j]) + T[row * TP + part * 16 + i * 8 + 2 * j]);
      float f1 = bfr(bf2f((u16)xv[2 * j + 1]) + T[row * TP + part * 16 + i * 8 + 2 * j + 1]);
      v[i * 8 + 2 * j] = f0; v[i * 8 + 2 * j + 1] = f1;
      s += f0 + f1;
      o[j] = pack2(f0, f1);
    }
    *(u32x4*)(xn + i * 8) = o;
  }
  s += __shfl_xor(s, 1); s += __shfl_xor(s, 2); s += __shfl_xor(s, 4);
  float mu = s * (1.f / 128.f), m2 = 0;
#pragma unroll
  for (int j = 0; j < 16; ++j) { float d = v[j] - mu; m2 += d * d; }
  m2 += __shfl_xor(m2, 1); m2 += __shfl_xor(m2, 2); m2 += __shfl_xor(m2, 4);
  if (part == 0) stats[(long)(m0 + row) * NPART + nblk] = make_float2(s, m2);
}

extern "C" void kernel_launch(void* const* d_in, const int* in_sizes, int n_in, void* d_out, int out_size,
                              void* d_ws, size_t ws_size, hipStream_t stream) {
  const u16* x_in = (const u16*)d_in[0];
  const u16* A_wqkv = (const u16*)d_in[1];
  const u16* A_wo = (const u16*)d_in[2];
  const u16* B_wqkv = (const u16*)d_in[4];
  const u16* B_wo = (const u16*)d_in[5];
  u16* out = (u16*)d_out;
  char* ws = (char*)d_ws;
  size_t off = 0;
  auto take = [&](size_t bytes) { char* p = ws + off; off += (bytes + 255) & ~(size_t)255; return p; };
  u16* xb0 = (u16*)take((size_t)S_ * D_ * 2);
  u16* xb1 = (u16*)take((size_t)S_ * D_ * 2);
  u16* qkv = (u16*)take((size_t)S_ * N3 * 2);
  u16* att = (u16*)take((size_t)S_ * D_ * 2);
  u16* Mm = (u16*)take((size_t)D_ * D_ * 2);
  float* kvp = (float*)take((size_t)16 * H_ * 1024 * 4);
  float* zp = (float*)take((size_t)16 * D_ * 4);
  float2* stats = (float2*)take((size_t)S_ * NPART * 8);
  float* csA = (float*)take((size_t)CSP * N3 * 4);
  float* csB = (float*)take((size_t)CSP * N3 * 4);
  float* zsum = (float*)take((size_t)S_ * 32 * 4);

  k_init<<<S_ / 4 + 2 * 12 * CSP, 256, 0, stream>>>(x_in, stats, A_wqkv, B_wqkv, csA, csB);
  k_qkv<0><<<256, 512, 0, stream>>>(x_in, A_wqkv, stats, csA, att, kvp, zp);
  k_lin_mid<<<384, 256, 0, stream>>>(kvp, zp, A_wo, Mm, att);
  k_out<false><<<256, 512, 0, stream>>>(att, Mm, x_in, xb0, stats, zsum);
  k_qkv<1><<<256, 512, 0, stream>>>(xb0, B_wqkv, stats, csB, qkv, kvp, zp);
  k_swa<<<512, 256, 0, stream>>>(qkv, att);
  k_out<false><<<256, 512, 0, stream>>>(att, B_wo, xb0, xb1, stats, zsum);
  k_qkv<0><<<256, 512, 0, stream>>>(xb1, A_wqkv, stats, csA, att, kvp, zp);
  k_lin_mid<<<384, 256, 0, stream>>>(kvp, zp, A_wo, Mm, att);
  k_out<false><<<256, 512, 0, stream>>>(att, Mm, xb1, xb0, stats, zsum);
  k_qkv<1><<<256, 512, 0, stream>>>(xb0, B_wqkv, stats, csB, qkv, kvp, zp);
  k_swa<<<512, 256, 0, stream>>>(qkv, att);
  k_out<false><<<256, 512, 0, stream>>>(att, B_wo, xb0, out, stats, zsum);
}
```

```cpp
#define ST_SC1
#include <hip/hip_runtime.h>
#include <hip/hip_bf16.h>
#include <stdint.h>
#include <type_traits>

constexpr int S_ = 2048, D_ = 1024, H_ = 32, DH = 32, WIN = 256;
constexpr int N3 = 3 * D_;
constexpr int NPART = 8;
constexpr int CSP = 8;

typedef __attribute__((ext_vector_type(8))) short bf16x8;
typedef __attribute__((ext_vector_type(4))) short s16x4;
typedef __attribute__((ext_vector_type(4))) float f32x4;
typedef __attribute__((ext_vector_type(16))) float f32x16;
typedef __attribute__((ext_vector_type(4))) unsigned u32x4;
typedef __attribute__((ext_vector_type(2))) unsigned u32x2;
typedef __attribute__((address_space(3))) s16x4* lds_s16x4_p;
typedef unsigned short u16;

#define DEV __device__ __forceinline__

DEV float bf2f(u16 v) { return __uint_as_float(((unsigned)v) << 16); }
typedef __attribute__((ext_vector_type(2))) float f32x2_t;
typedef __attribute__((ext_vector_type(2))) __bf16 bf16x2_t;
DEV unsigned pack2(float lo, float hi) {
  f32x2_t v = {lo, hi};
  bf16x2_t b = __builtin_convertvector(v, bf16x2_t);
  return __builtin_bit_cast(unsigned, b);
}
DEV u16 f2bf(float f) { return (u16)(pack2(f, f) & 0xffffu); }
DEV float bfr(float f) { return __uint_as_float(pack2(f, f) << 16); }
DEV float phi_f(float t) { return (t > 0.f ? t + 1.f : __expf(t)) + 1e-6f; }

DEV void st16(void* p, u32x4 v) {
  asm volatile("global_store_dwordx4 %0, %1, off sc1\n\ts_nop 1" ::"v"(p), "v"(v) : "memory");
}
DEV s16x4 tr_read(const char* p) {
  return __builtin_amdgcn_ds_read_tr16_b64_v4i16((lds_s16x4_p)(p));
}
DEV bf16x8 cat8(s16x4 a, s16x4 b) { return __builtin_shufflevector(a, b, 0, 1, 2, 3, 4, 5, 6, 7); }

#define WAITV(n) asm volatile("s_waitcnt vmcnt(" #n ")" ::: "memory")
template <int N> DEV void waitv() { asm volatile("s_waitcnt vmcnt(%0)" ::"n"(N) : "memory"); }
DEV void wait_lgkm0() { __builtin_amdgcn_s_waitcnt(0xC07F); }
DEV void raw_barrier() {
  asm volatile("" ::: "memory");
  wait_lgkm0();
  __builtin_amdgcn_s_barrier();
  asm volatile("" ::: "memory");
}

template <int BM, int NSEG, int WGM, int WGN, int NSTAGE>
struct GemmCfg {
  static constexpr int NW = WGM * WGN;
  static constexpr int NT_ = NW * 64;
  static constexpr int BN = NSEG * 64;
  static constexpr int WTM = BM / WGM, WTN = BN / WGN;
  static constexpr int MB = WTM / 16, NB = WTN / 16;
  static constexpr int APIECES = BM / 8, BPIECES = NSEG * 8;
  static constexpr int PPW = (APIECES + BPIECES) / NW;
  static constexpr int STAGE_BYTES = (APIECES + BPIECES) * 1024;
  static constexpr int LDS_BYTES = STAGE_BYTES * NSTAGE;
  static_assert((APIECES + BPIECES) % NW == 0, "pieces");
};

template <int BM, int NSEG, int WGM, int WGN, int NSTAGE, bool SCALE, typename PRE>
DEV void gemm_mainloop(PRE pre, const float* sinv  , const u16* __restrict__ A, int lda, const u16* __restrict__ B, int ldb,
                       int m0, int colbase0, int segstride, char* smem,
                       f32x4 (&acc)[GemmCfg<BM, NSEG, WGM, WGN, NSTAGE>::MB][GemmCfg<BM, NSEG, WGM, WGN, NSTAGE>::NB]) {
  using C = GemmCfg<BM, NSEG, WGM, WGN, NSTAGE>;
  constexpr int KT = D_ / 64;
  const int tid = threadIdx.x, lane = tid & 63;
  const int wid = __builtin_amdgcn_readfirstlane(tid >> 6);
  const int wm = wid / WGN, wn = wid % WGN;

  const u16* gp[C::PPW];
  int gstep[C::PPW];

#pragma unroll
  for (int i = 0; i < C::PPW; ++i) {
    int p = wid * C::PPW + i;
    if (p < C::APIECES) {
      int row = p * 8 + (lane >> 3), slot = lane & 7;
      int chunk = slot ^ ((row >> 1) & 7);
      gstep[i] = 64;
      gp[i] = A + (long)(m0 + row) * lda + chunk * 8;
    } else {
      int pb = p - C::APIECES;
      int seg = pb >> 3, k = (pb & 7) * 8 + (lane >> 3), slot = lane & 7;
      int f = ((k >> 1) & 1) | (((k >> 3) & 1) << 1);
      int chunk = slot ^ (f << 1);
      int cb = colbase0 + seg * segstride;
      gstep[i] = 64 * ldb;
      gp[i] = B + (long)k * ldb + cb + chunk * 8;
    }
  }
  auto issue_piece = [&](int stage, int i) {
    char* base = smem + stage * C::STAGE_BYTES + wid * C::PPW * 1024;
    __builtin_amdgcn_global_load_lds((const unsigned*)gp[i], (unsigned*)(base + i * 1024), 16, 0, 0);
    gp[i] += gstep[i];
  };

  const int l15 = lane & 15, g = lane >> 4;
  const int a_off = (wm * C::WTM + l15) * 128;
  const int a_sw = (l15 >> 1);
  const int q = (lane & 15) >> 2, p = lane & 3;
  const int fB = ((q >> 1) & 1) | ((g & 1) << 1);
  const int b_base = C::APIECES * 1024 + (8 * g + q) * 128 + (p & 1) * 8;

#pragma unroll
  for (int s = 0; s < NSTAGE; ++s)
#pragma unroll
    for (int i = 0; i < C::PPW; ++i) issue_piece(s, i);
  pre();

  bf16x8 af[2][C::MB];
  s16x4 bl[2][C::NB][2];
  constexpr int NM = C::NB * C::MB;
  constexpr int NRD = C::NB * 2 + C::MB;
  auto read1 = [&](int stage, int ks, int set, int k) {
    const char* sb = smem + stage * C::STAGE_BYTES;
    if (k < C::NB * 2) {
      int nb = k >> 1, hf = k & 1;
      int gnb = wn * C::NB + nb;
      int seg = gnb >> 2, c = gnb & 3;
      const char* bp = sb + b_base + seg * 8192 + ks * 4096 + ((((2 * c + (p >> 1)) ^ (fB << 1))) << 4);
      bl[set][nb][hf] = tr_read(bp + hf * 512);
    } else {
      int mb = k - C::NB * 2;
      af[set][mb] = *(const bf16x8*)(sb + a_off + mb * 2048 + (((ks * 4 + g) ^ a_sw) << 4));
    }
  };
  auto phase = [&](int set, bool rd, int rstage, int rks, bool iss, int dstage, int head) {
    float scl[C::MB];
    f32x4 t4[C::MB][C::NB];
    if (SCALE) {
      asm volatile("" ::: "memory");
#pragma unroll
      for (int mb = 0; mb < C::MB; ++mb) scl[mb] = sinv[(wm * C::WTM + mb * 16 + l15) * 33 + head];
    }
    constexpr int NSIDE_MAX = NRD + C::PPW;
    constexpr int PER = (NSIDE_MAX + NM - 1) / NM;
    const int nrd = rd ? NRD : 0;
    const int nside = nrd + (iss ? C::PPW : 0);
#pragma unroll
    for (int nb = 0; nb < C::NB; ++nb)
#pragma unroll
      for (int mb = 0; mb < C::MB; ++mb) {
        const int idx = nb * C::MB + mb;
        if (SCALE) {
          t4[mb][nb] = __builtin_amdgcn_mfma_f32_16x16x32_bf16(cat8(bl[set][nb][0], bl[set][nb][1]), af[set][mb], (f32x4){0, 0, 0, 0}, 0, 0, 0);
        } else {
          acc[mb][nb] = __builtin_amdgcn_mfma_f32_16x16x32_bf16(cat8(bl[set][nb][0], bl[set][nb][1]), af[set][mb], acc[mb][nb], 0, 0, 0);
        }
        __builtin_amdgcn_sched_barrier(0);
#pragma unroll
        for (int e = 0; e < PER; ++e) {
          const int k = idx * PER + e;
          if (k < nrd) read1(rstage, rks, set ^ 1, k);
          else if (k < nside) issue_piece(dstage, k - nrd);
        }
        __builtin_amdgcn_sched_barrier(0);
      }
    if (SCALE) {
#pragma unroll
      for (int nb = 0; nb < C::NB; ++nb)
#pragma unroll
        for (int mb = 0; mb < C::MB; ++mb) {
#pragma unroll
          for (int r = 0; r < 4; ++r) acc[mb][nb][r] = fmaf(t4[mb][nb][r], scl[mb], acc[mb][nb][r]);
          asm volatile("" : "+v"(acc[mb][nb]));
        }
    }
  };
  const bool grpA = wid < C::NW / 2;

  waitv<C::PPW*(NSTAGE - 1)>();
  raw_barrier();
#pragma unroll
  for (int k = 0; k < NRD; ++k) read1(0, 0, 0, k);
  auto run = [&](auto GA) {
    constexpr bool ga = decltype(GA)::value;
#pragma unroll
    for (int kt = 0; kt < KT; ++kt) {
      const int cs = kt % NSTAGE, ns = (kt + 1) % NSTAGE, ps = (kt + NSTAGE - 1) % NSTAGE;
      __builtin_amdgcn_sched_barrier(0);
      phase(0, true, cs, 1, ga && kt >= 1 && kt - 1 + NSTAGE < KT, ps, 2 * kt);
      if (kt + 1 < KT) {
        if (kt + NSTAGE - 1 < KT) waitv<C::PPW*(NSTAGE - 2)>(); else waitv<0>();
        raw_barrier();
        __builtin_amdgcn_sched_barrier(0);
        phase(1, true, ns, 0, !ga && kt + NSTAGE < KT, cs, 2 * kt + 1);
      } else {
        phase(1, false, 0, 0, false, 0, 2 * kt + 1);
      }
    }
    __builtin_amdgcn_sched_barrier(0);
  };
  (void)grpA;
  run(std::integral_constant<bool, false>{});
  raw_barrier();
}

__global__ __launch_bounds__(256) void k_init(const u16* __restrict__ x, float2* __restrict__ stats,
                                              const u16* __restrict__ wA, const u16* __restrict__ wB,
                                              float* __restrict__ csA, float* __restrict__ csB,
                                              const u16* __restrict__ woA, const u16* __restrict__ woB, unsigned* __restrict__ junk) {
  const int tid = threadIdx.x;
  if (blockIdx.x >= S_ / 4 + 2 * 12 * CSP) {
    int b = blockIdx.x - (S_ / 4 + 2 * 12 * CSP);
    const u16* w = (b & 32) ? woB : woA;
    const u32x4* src = (const u32x4*)(w + (long)(b & 31) * 32768) + tid;
    u32x4 v[16];
#pragma unroll
    for (int i = 0; i < 16; ++i) v[i] = src[i * 256];
    unsigned a = 0;
#pragma unroll
    for (int i = 0; i < 16; ++i) a ^= v[i][0] ^ v[i][1] ^ v[i][2] ^ v[i][3];
    junk[b * 256 + tid] = a;
    return;
  }
  if (blockIdx.x < S_ / 4) {
    int row = blockIdx.x * 4 + (tid >> 6), lane = tid & 63;
    const bf16x8* px = (const bf16x8*)(x + (long)row * D_ + lane * 16);
    bf16x8 v0 = px[0], v1 = px[1];
    float f[16];
#pragma unroll
    for (int j = 0; j < 8; ++j) { f[j] = bf2f((u16)v0[j]); f[8 + j] = bf2f((u16)v1[j]); }
    float s = 0;
#pragma unroll
    for (int j = 0; j < 16; ++j) s += f[j];
    s += __shfl_xor(s, 1); s += __shfl_xor(s, 2); s += __shfl_xor(s, 4);
    float mu = s * (1.f / 128.f), m2 = 0;
#pragma unroll
    for (int j = 0; j < 16; ++j) { float d = f[j] - mu; m2 += d * d; }
    m2 += __shfl_xor(m2, 1); m2 += __shfl_xor(m2, 2); m2 += __shfl_xor(m2, 4);
    if ((lane & 7) == 0) stats[row * NPART + (lane >> 3)] = make_float2(s, m2);
  } else {
    int b = blockIdx.x - S_ / 4;
    int wsel = b / (12 * CSP); b %= 12 * CSP;
    int cb = b / CSP, rs = b % CSP;
    const u16* w = wsel ? wB : wA;
    float* cs = wsel ? csB : csA;
    __shared__ float red[8][256];
    int cg = tid & 31, rg = tid >> 5;
    float a[8] = {0, 0, 0, 0, 0, 0, 0, 0};
    bf16x8 v[16];
#pragma unroll
    for (int i = 0; i < 16; ++i) v[i] = *(const bf16x8*)(w + (long)(rs * 128 + rg + i * 8) * N3 + cb * 256 + cg * 8);
#pragma unroll
    for (int i = 0; i < 16; ++i)
#pragma unroll
      for (int j = 0; j < 8; ++j) a[j] += bf2f((u16)v[i][j]);
#pragma unroll
    for (int j = 0; j < 8; ++j) red[rg][cg * 8 + j] = a[j];
    __syncthreads();
    float t = 0;
#pragma unroll
    for (int i = 0; i < 8; ++i) t += red[i][tid];
    cs[rs * N3 + cb * 256 + tid] = t;
  }
}

using G1C = GemmCfg<128, 3, 2, 4, 3>;
constexpr int G1_TP = 200;

template <int MODE>
__global__ __launch_bounds__(512, 2) void k_qkv(const u16* __restrict__ x, const u16* __restrict__ w,
                                                const float2* __restrict__ stats, const float* __restrict__ csp,
                                                u16* __restrict__ out  , float* __restrict__ kvp,
                                                float* __restrict__ zp) {
  __shared__ __attribute__((aligned(16))) char smem[G1C::LDS_BYTES + 2048];
  float* s_mean = (float*)(smem + G1C::LDS_BYTES);
  float* s_rstd = s_mean + 128;
  float* s_cs = s_rstd + 128;
  const int tid = threadIdx.x, lane = tid & 63;
  int bid = blockIdx.x;
  int xcd = bid & 7, idx = bid >> 3;
  int mblk = (xcd & 1) * 8 + (idx & 7), nblk = (xcd >> 1) * 4 + (idx >> 3);
  const int m0 = mblk * 128;

  f32x4 acc[G1C::MB][G1C::NB];
#pragma unroll
  for (int i = 0; i < G1C::MB; ++i)
#pragma unroll
    for (int j = 0; j < G1C::NB; ++j) acc[i][j] = (f32x4){0, 0, 0, 0};

  auto pre = [&]() {
  if (tid < 128) {
    const float2* sp = stats + (long)(m0 + tid) * NPART;
    float2 pr[NPART];
#pragma unroll
    for (int i = 0; i < NPART; ++i) pr[i] = sp[i];
    float s = 0;
#pragma unroll
    for (int i = 0; i < NPART; ++i) s += pr[i].x;
    float mean = s * (1.f / D_), m2 = 0;
#pragma unroll
    for (int i = 0; i < NPART; ++i) { float d = pr[i].x * (1.f / 128.f) - mean; m2 += pr[i].y + 128.f * d * d; }
    s_mean[tid] = mean;
    s_rstd[tid] = rsqrtf(m2 * (1.f / D_) + 1e-5f);
  } else if (tid < 128 + 192) {
    int c = tid - 128;
    int col = (c >> 6) * D_ + nblk * 64 + (c & 63);
    float t = 0;
#pragma unroll
    for (int i = 0; i < CSP; ++i) t += csp[i * N3 + col];
    s_cs[c] = t;
  }

  };
  gemm_mainloop<128, 3, 2, 4, 3, false>(pre, nullptr, x, D_, w, N3, m0, nblk * 64, D_, smem, acc);

  const int wid = tid >> 6, wm = wid / 4, wn = wid % 4;
  u16* T = (u16*)smem;
  const int l15 = lane & 15, g = lane >> 4;
#pragma unroll
  for (int mb = 0; mb < G1C::MB; ++mb) {
    const int row = wm * G1C::WTM + mb * 16 + l15;
    const float mean = s_mean[row], rstd = s_rstd[row];
#pragma unroll
    for (int nb = 0; nb < G1C::NB; ++nb) {
      const int col = wn * G1C::WTN + nb * 16 + g * 4;
      const f32x4 csv = *(const f32x4*)(s_cs + col);
      const int seg = col >> 6;
      float y[4];
#pragma unroll
      for (int r = 0; r < 4; ++r) {
        y[r] = (acc[mb][nb][r] - mean * csv[r]) * rstd;
        if (MODE == 0 && seg < 2) y[r] = phi_f(bfr(y[r]));
      }
      u32x2 w2;
      w2[0] = pack2(y[0], y[1]); w2[1] = pack2(y[2], y[3]);
      *(u32x2*)(T + row * G1_TP + col) = w2;
    }
  }
  __syncthreads();
  if (MODE == 1) {
#pragma unroll
    for (int i = 0; i < 6; ++i) {
      const int row = tid >> 2, part = tid & 3;
      u32x4 v = *(const u32x4*)(T + row * G1_TP + i * 32 + part * 8);
      st16(out + ((long)((i >> 1) * H_ + nblk * 2 + (i & 1)) * S_ + m0 + row) * DH + part * 8, v);
    }
  } else {
#pragma unroll
    for (int i = 0; i < 2; ++i) {
      int ch = tid + i * 512;
      int row = ch >> 3, c = ch & 7;
      u32x4 v = *(const u32x4*)(T + row * G1_TP + c * 8);
      st16(out + (long)(m0 + row) * D_ + nblk * 64 + c * 8, v);
    }
    int hh = wid >> 2, dt = (wid >> 1) & 1, et = wid & 1;
    int q = (lane & 15) >> 2, p = lane & 3;
    f32x4 kv = {0, 0, 0, 0}, zz = {0, 0, 0, 0};
    bf16x8 ones;
#pragma unroll
    for (int j = 0; j < 8; ++j) ones[j] = (short)0x3F80;
#pragma unroll
    for (int ks = 0; ks < 4; ++ks) {
      int r0 = ks * 32 + 8 * g + q;
      const char* pa = (const char*)(T + r0 * G1_TP + 64 + hh * 32 + dt * 16 + 4 * p);
      const char* pb = (const char*)(T + r0 * G1_TP + 128 + hh * 32 + et * 16 + 4 * p);
      bf16x8 a = cat8(tr_read(pa), tr_read(pa + 4 * G1_TP * 2));
      bf16x8 b = cat8(tr_read(pb), tr_read(pb + 4 * G1_TP * 2));
      kv = __builtin_amdgcn_mfma_f32_16x16x32_bf16(a, b, kv, 0, 0, 0);
      zz = __builtin_amdgcn_mfma_f32_16x16x32_bf16(a, ones, zz, 0, 0, 0);
    }
    int head = nblk * 2 + hh;
    float* kp = kvp + ((long)(mblk * H_ + head) * 32) * 32;
#pragma unroll
    for (int r = 0; r < 4; ++r) {
      int d = dt * 16 + g * 4 + r;
      kp[d * 32 + et * 16 + l15] = kv[r];
      if (et == 0 && l15 == 0) zp[mblk * D_ + head * 32 + d] = zz[r];
    }
  }
}

__global__ __launch_bounds__(256) void k_lin_mid(const float* __restrict__ kvp, const float* __restrict__ zp,
                                                 const u16* __restrict__ wo, u16* __restrict__ M,
                                                 u16* __restrict__ pq  ) {
  constexpr int WP = 264;
  constexpr int KP = 40;
  __shared__ __attribute__((aligned(16))) u16 sw[32 * WP];
  __shared__ __attribute__((aligned(16))) u16 skv[32 * KP];
  const int tid = threadIdx.x, lane = tid & 63, wid = tid >> 6;
  if (blockIdx.x >= 128) {
    const int r0 = (blockIdx.x - 128) * 8 + wid * 2;
    float* zs = (float*)sw;
    f32x4 zpr[16];
#pragma unroll
    for (int mb = 0; mb < 16; ++mb) zpr[mb] = *(const f32x4*)(zp + mb * D_ + tid * 4);
    bf16x8 v[2][2];
#pragma unroll
    for (int r = 0; r < 2; ++r) {
      const u16* pp = pq + (long)(r0 + r) * D_ + lane * 16;
      v[r][0] = *(const bf16x8*)(pp);
      v[r][1] = *(const bf16x8*)(pp + 8);
    }
    f32x4 zs4 = {0, 0, 0, 0};
#pragma unroll
    for (int mb = 0; mb < 16; ++mb) zs4 += zpr[mb];
    *(f32x4*)(zs + tid * 4) = zs4;
    __syncthreads();
    f32x4 z4[4];
#pragma unroll
    for (int i = 0; i < 4; ++i) z4[i] = *(const f32x4*)(zs + lane * 16 + i * 4);
#pragma unroll
    for (int r = 0; r < 2; ++r) {
      float den = 0;
#pragma unroll
      for (int i = 0; i < 2; ++i)
#pragma unroll
        for (int j = 0; j < 4; ++j)
          den += bf2f((u16)v[r][i][j]) * z4[2 * i][j] + bf2f((u16)v[r][i][4 + j]) * z4[2 * i + 1][j];
      den += __shfl_xor(den, 1);
      const float iv = 1.f / (den + 1e-6f);
      u16* pp = pq + (long)(r0 + r) * D_ + lane * 16;
#pragma unroll
      for (int i = 0; i < 2; ++i) {
        u32x4 o;
#pragma unroll
        for (int j = 0; j < 4; ++j) o[j] = pack2(bf2f((u16)v[r][i][2 * j]) * iv, bf2f((u16)v[r][i][2 * j + 1]) * iv);
        *(u32x4*)(pp + i * 8) = o;
      }
    }
    return;
  }
  const int h = blockIdx.x >> 2, nq = blockIdx.x & 3;
  f32x4 pr[16];
#pragma unroll
  for (int mb = 0; mb < 16; ++mb) pr[mb] = *(const f32x4*)(kvp + ((long)(mb * H_ + h) * 1024) + tid * 4);
  u32x4 wv[4];
#pragma unroll
  for (int i = 0; i < 4; ++i)
    wv[i] = *(const u32x4*)(wo + (long)(h * 32 + (tid >> 5) + i * 8) * D_ + nq * 256 + (tid & 31) * 8);
  f32x4 s4 = {0, 0, 0, 0};
#pragma unroll
  for (int mb = 0; mb < 16; ++mb) s4 += pr[mb];
  {
    u32x2 w2;
    w2[0] = pack2(s4[0], s4[1]); w2[1] = pack2(s4[2], s4[3]);
    *(u32x2*)(skv + (tid >> 3) * KP + (tid & 7) * 4) = w2;
  }
#pragma unroll
  for (int i = 0; i < 4; ++i) *(u32x4*)(sw + ((tid >> 5) + i * 8) * WP + (tid & 31) * 8) = wv[i];
  __syncthreads();
  const int l15 = lane & 15, g = lane >> 4, q = (lane & 15) >> 2, p = lane & 3;
  bf16x8 kf[2];
#pragma unroll
  for (int db = 0; db < 2; ++db) kf[db] = *(const bf16x8*)(skv + (db * 16 + l15) * KP + g * 8);
#pragma unroll
  for (int nb = 0; nb < 4; ++nb) {
    const int n0 = wid * 64 + nb * 16;
    const char* pa = (const char*)(sw + (8 * g + q) * WP + n0 + 4 * p);
    bf16x8 wf = cat8(tr_read(pa), tr_read(pa + 4 * WP * 2));
#pragma unroll
    for (int db = 0; db < 2; ++db) {
      f32x4 d4 = __builtin_amdgcn_mfma_f32_16x16x32_bf16(wf, kf[db], (f32x4){0, 0, 0, 0}, 0, 0, 0);
      u32x2 w2;
      w2[0] = pack2(d4[0], d4[1]); w2[1] = pack2(d4[2], d4[3]);
      *(u32x2*)(M + (long)(h * 32 + db * 16 + l15) * D_ + nq * 256 + n0 + 4 * g) = w2;
    }
  }
}

DEV int crow(int r, int hi) { return (r & 3) + 8 * (r >> 2) + 4 * hi; }

__global__ __launch_bounds__(256, 2) void k_swa(const u16* __restrict__ qkv, u16* __restrict__ att) {
  __shared__ __attribute__((aligned(16))) char sK[384 * 64];
  __shared__ __attribute__((aligned(16))) char sV[384 * 64];
  const int tid = threadIdx.x, lane = tid & 63, wid = tid >> 6;
  const int h = blockIdx.x & 31, qb = blockIdx.x >> 5;
  const int q0 = qb * 128, kbase = q0 - 256;
#pragma unroll
  for (int i = 0; i < 6; ++i) {
    int ch = tid + i * 256;
    int r = ch >> 2, c = ch & 3;
    int key = kbase + r;
    u32x4 kv4 = {0, 0, 0, 0}, vv4 = {0, 0, 0, 0};
    if (key >= 0) {
      kv4 = *(const u32x4*)(qkv + ((long)(H_ + h) * S_ + key) * DH + c * 8);
      vv4 = *(const u32x4*)(qkv + ((long)(2 * H_ + h) * S_ + key) * DH + c * 8);
    }
    *(u32x4*)(sK + r * 64 + ((c ^ ((r >> 2) & 3)) << 4)) = kv4;
    *(u32x4*)(sV + r * 64 + (c << 4)) =vv4;
  }
  const int l31 = lane & 31, hi = lane >> 5;
  const int qw = q0 + wid * 32;
  const int qi = qw + l31;
  bf16x8 qf[2];
#pragma unroll
  for (int ks = 0; ks < 2; ++ks) qf[ks] = *(const bf16x8*)(qkv + ((long)h * S_ + qi) * DH + ks * 16 + hi * 8);
  __syncthreads();

  typedef __attribute__((ext_vector_type(2))) float f32x2;
  const int kt0 = (qw >= 256) ? 0 : (256 - qw) / 32;
  f32x16 sc[9];
  const float scale_l2 = 0.17677669529663687f * 1.4426950408889634f;
  float mx = -3.0e38f;
  const f32x16 zero16 = {0, 0, 0, 0, 0, 0, 0, 0, 0, 0, 0, 0, 0, 0, 0, 0};
#pragma unroll
  for (int kt = 0; kt < 9; ++kt) {
    f32x16 a16;
    if (kt >= kt0) {
      int r = wid * 32 + kt * 32 + l31;
      bf16x8 kf0 = *(const bf16x8*)(sK + r * 64 + ((hi ^ ((r >> 2) & 3)) << 4));
      bf16x8 kf1 = *(const bf16x8*)(sK + r * 64 + (((2 + hi) ^ ((r >> 2) & 3)) << 4));
      a16 = __builtin_amdgcn_mfma_f32_32x32x16_bf16(kf0, qf[0], zero16, 0, 0, 0);
      a16 = __builtin_amdgcn_mfma_f32_32x32x16_bf16(kf1, qf[1], a16, 0, 0, 0);
      if (kt == 0) {
#pragma unroll
        for (int r2 = 0; r2 < 16; ++r2) a16[r2] = (crow(r2, hi) > l31) ? a16[r2] : -3.0e38f;
      }
      if (kt == 8) {
#pragma unroll
        for (int r2 = 0; r2 < 16; ++r2) a16[r2] = (crow(r2, hi) <= l31) ? a16[r2] : -3.0e38f;
      }
#pragma unroll
      for (int r2 = 0; r2 < 16; r2 += 2) mx = fmaxf(fmaxf(mx, a16[r2]), a16[r2 + 1]);
    } else {
#pragma unroll
      for (int i = 0; i < 16; ++i) a16[i] = -3.0e38f;
    }
    sc[kt] = a16;
  }
  mx = fmaxf(mx, __shfl_xor(mx, 32));
  const float mxs = -mx * scale_l2;
  const f32x2 sc2 = {scale_l2, scale_l2}, mx2 = {mxs, mxs};
  f32x2 sum2 = {0, 0};
#pragma unroll
  for (int kt = 0; kt < 9; ++kt)
#pragma unroll
    for (int r = 0; r < 16; r += 2) {
      f32x2 t = {sc[kt][r], sc[kt][r + 1]};
      t = t * sc2 + mx2;
      f32x2 e = {__builtin_amdgcn_exp2f(t[0]), __builtin_amdgcn_exp2f(t[1])};
      sc[kt][r] = e[0]; sc[kt][r + 1] = e[1];
      sum2 += e;
    }
  float sum = sum2[0] + sum2[1];
  sum += __shfl_xor(sum, 32);
  f32x16 o = zero16;
  const int q4 = (lane & 15) >> 2, p4 = lane & 3, blk = (lane >> 4) & 1;
#pragma unroll
  for (int kt = 0; kt < 9; ++kt) {
#pragma unroll
    for (int s = 0; s < 2; ++s) {
      u32x4 pk;
#pragma unroll
      for (int j = 0; j < 4; ++j) pk[j] = pack2(sc[kt][8 * s + 2 * j], sc[kt][8 * s + 2 * j + 1]);
      bf16x8 pf = __builtin_bit_cast(bf16x8, pk);
      int r0 = wid * 32 + kt * 32 + 16 * s + 4 * hi + q4;
      const char* vp = sV + r0 * 64 + (16 * blk + 4 * p4) * 2;
      bf16x8 vf = cat8(tr_read(vp), tr_read(vp + 8 * 64));
      o = __builtin_amdgcn_mfma_f32_32x32x16_bf16(vf, pf, o, 0, 0, 0);
    }
  }
  float inv = 1.f / sum;
  u16* op = att + (long)qi * D_ + h * 32;
#pragma unroll
  for (int gq = 0; gq < 4; ++gq) {
    u32x2 w2;
    w2[0] = pack2(o[4 * gq + 0] * inv, o[4 * gq + 1] * inv);
    w2[1] = pack2(o[4 * gq + 2] * inv, o[4 * gq + 3] * inv);
    *(u32x2*)(op + 8 * gq + 4 * hi) = w2;
  }
}

using G2C = GemmCfg<64, 2, 2, 4, 4>;

template <bool LIN>
__global__ __launch_bounds__(512, 2) void k_out(const u16* __restrict__ a, const u16* __restrict__ w,
                                                const u16* __restrict__ xold, u16* __restrict__ xnew,
                                                float2* __restrict__ stats, const float* __restrict__ zsum) {
  __shared__ __attribute__((aligned(16))) char smem[G2C::LDS_BYTES + 64 * 33 * 4];
  float* sinv = (float*)(smem + G2C::LDS_BYTES);
  const int tid = threadIdx.x, lane = tid & 63;
  int bid = blockIdx.x;
  int xcd = bid & 7, idx = bid >> 3;
  int mblk = xcd * 4 + (idx & 3), nblk = idx >> 2;
  const int m0 = mblk * 64;
  f32x4 acc[G2C::MB][G2C::NB];
#pragma unroll
  for (int i = 0; i < G2C::MB; ++i)
#pragma unroll
    for (int j = 0; j < G2C::NB; ++j) acc[i][j] = (f32x4){0, 0, 0, 0};
  auto pre = [&]() {
    if (LIN) {
      f32x4 v4 = *(const f32x4*)(zsum + (long)m0 * 32 + tid * 4);
      float* dst = sinv + (tid >> 3) * 33 + (tid & 7) * 4;
#pragma unroll
      for (int j = 0; j < 4; ++j) dst[j] = v4[j];
    }
  };
  gemm_mainloop<64, 2, 2, 4, 4, LIN>(pre, sinv, a, D_, w, D_, m0, nblk * 128, 64, smem, acc);

  const int wid = tid >> 6, wm = wid / 4, wn = wid % 4;
  float* T = (float*)smem;
  constexpr int TP = 132;
  const int l15 = lane & 15, g = lane >> 4;
#pragma unroll
  for (int mb = 0; mb < G2C::MB; ++mb)
#pragma unroll
    for (int nb = 0; nb < G2C::NB; ++nb)
      *(f32x4*)(T + (wm * 32 + mb * 16 + l15) * TP + wn * 32 + nb * 16 + g * 4) = acc[mb][nb];
  __syncthreads();
  int row = tid >> 3, part = tid & 7;
  const u16* xo = xold + (long)(m0 + row) * D_ + nblk * 128 + part * 16;
  u16* xn = xnew + (long)(m0 + row) * D_ + nblk * 128 + part * 16;
  float v[16];
  float s = 0;
#pragma unroll
  for (int i = 0; i < 2; ++i) {
    bf16x8 xv = *(const bf16x8*)(xo + i * 8);
    u32x4 o;
#pragma unroll
    for (int j = 0; j < 4; ++j) {
      float f0 = bfr(bf2f((u16)xv[2 * j]) + T[row * TP + part * 16 + i * 8 + 2 * j]);
      float f1 = bfr(bf2f((u16)xv[2 * j + 1]) + T[row * TP + part * 16 + i * 8 + 2 * j + 1]);
      v[i * 8 + 2 * j] = f0; v[i * 8 + 2 * j + 1] = f1;
      s += f0 + f1;
      o[j] = pack2(f0, f1);
    }
    *(u32x4*)(xn + i * 8) = o;
  }
  s += __shfl_xor(s, 1); s += __shfl_xor(s, 2); s += __shfl_xor(s, 4);
  float mu = s * (1.f / 128.f), m2 = 0;
#pragma unroll
  for (int j = 0; j < 16; ++j) { float d = v[j] - mu; m2 += d * d; }
  m2 += __shfl_xor(m2, 1); m2 += __shfl_xor(m2, 2); m2 += __shfl_xor(m2, 4);
  if (part == 0) stats[(long)(m0 + row) * NPART + nblk] = make_float2(s, m2);
}

extern "C" void kernel_launch(void* const* d_in, const int* in_sizes, int n_in, void* d_out, int out_size,
                              void* d_ws, size_t ws_size, hipStream_t stream) {
  const u16* x_in = (const u16*)d_in[0];
  const u16* A_wqkv = (const u16*)d_in[1];
  const u16* A_wo = (const u16*)d_in[2];
  const u16* B_wqkv = (const u16*)d_in[4];
  const u16* B_wo = (const u16*)d_in[5];
  u16* out = (u16*)d_out;
  char* ws = (char*)d_ws;
  size_t off = 0;
  auto take = [&](size_t bytes) { char* p = ws + off; off += (bytes + 255) & ~(size_t)255; return p; };
  u16* xb0 = (u16*)take((size_t)S_ * D_ * 2);
  u16* xb1 = (u16*)take((size_t)S_ * D_ * 2);
  u16* qkv = (u16*)take((size_t)S_ * N3 * 2);
  u16* att = (u16*)take((size_t)S_ * D_ * 2);
  u16* Mm = (u16*)take((size_t)D_ * D_ * 2);
  float* kvp = (float*)take((size_t)16 * H_ * 1024 * 4);
  float* zp = (float*)take((size_t)16 * D_ * 4);
  float2* stats = (float2*)take((size_t)S_ * NPART * 8);
  float* csA = (float*)take((size_t)CSP * N3 * 4);
  float* csB = (float*)take((size_t)CSP * N3 * 4);
  float* zsum = (float*)take((size_t)S_ * 32 * 4);

  unsigned* junk = (unsigned*)take((size_t)64 * 256 * 4);
  k_init<<<S_ / 4 + 2 * 12 * CSP + 64, 256, 0, stream>>>(x_in, stats, A_wqkv, B_wqkv, csA, csB, A_wo, B_wo, junk);
  k_qkv<0><<<256, 512, 0, stream>>>(x_in, A_wqkv, stats, csA, att, kvp, zp);
  k_lin_mid<<<384, 256, 0, stream>>>(kvp, zp, A_wo, Mm, att);
  k_out<false><<<256, 512, 0, stream>>>(att, Mm, x_in, xb0, stats, zsum);
  k_qkv<1><<<256, 512, 0, stream>>>(xb0, B_wqkv, stats, csB, qkv, kvp, zp);
  k_swa<<<512, 256, 0, stream>>>(qkv, att);
  k_out<false><<<256, 512, 0, stream>>>(att, B_wo, xb0, xb1, stats, zsum);
  k_qkv<0><<<256, 512, 0, stream>>>(xb1, A_wqkv, stats, csA, att, kvp, zp);
  k_lin_mid<<<384, 256, 0, stream>>>(kvp, zp, A_wo, Mm, att);
  k_out<false><<<256, 512, 0, stream>>>(att, Mm, xb1, xb0, stats, zsum);
  k_qkv<1><<<256, 512, 0, stream>>>(xb0, B_wqkv, stats, csB, qkv, kvp, zp);
  k_swa<<<512, 256, 0, stream>>>(qkv, att);
  k_out<false><<<256, 512, 0, stream>>>(att, B_wo, xb0, out, stats, zsum);
}
```

```cpp
#define ST_SC1
#include <hip/hip_runtime.h>
#include <hip/hip_bf16.h>
#include <stdint.h>
#include <type_traits>

constexpr int S_ = 2048, D_ = 1024, H_ = 32, DH = 32, WIN = 256;
constexpr int N3 = 3 * D_;
constexpr int NPART = 8;
constexpr int CSP = 8;

typedef __attribute__((ext_vector_type(8))) short bf16x8;
typedef __attribute__((ext_vector_type(4))) short s16x4;
typedef __attribute__((ext_vector_type(4))) float f32x4;
typedef __attribute__((ext_vector_type(16))) float f32x16;
typedef __attribute__((ext_vector_type(4))) unsigned u32x4;
typedef __attribute__((ext_vector_type(2))) unsigned u32x2;
typedef __attribute__((address_space(3))) s16x4* lds_s16x4_p;
typedef unsigned short u16;

#define DEV __device__ __forceinline__

DEV float bf2f(u16 v) { return __uint_as_float(((unsigned)v) << 16); }
typedef __attribute__((ext_vector_type(2))) float f32x2_t;
typedef __attribute__((ext_vector_type(2))) __bf16 bf16x2_t;
DEV unsigned pack2(float lo, float hi) {
  f32x2_t v = {lo, hi};
  bf16x2_t b = __builtin_convertvector(v, bf16x2_t);
  return __builtin_bit_cast(unsigned, b);
}
DEV u16 f2bf(float f) { return (u16)(pack2(f, f) & 0xffffu); }
DEV float bfr(float f) { return __uint_as_float(pack2(f, f) << 16); }
DEV float phi_f(float t) { return (t > 0.f ? t + 1.f : __expf(t)) + 1e-6f; }

DEV void st16(void* p, u32x4 v) {
  asm volatile("global_store_dwordx4 %0, %1, off sc1\n\ts_nop 1" ::"v"(p), "v"(v) : "memory");
}
DEV s16x4 tr_read(const char* p) {
  return __builtin_amdgcn_ds_read_tr16_b64_v4i16((lds_s16x4_p)(p));
}
DEV bf16x8 cat8(s16x4 a, s16x4 b) { return __builtin_shufflevector(a, b, 0, 1, 2, 3, 4, 5, 6, 7); }

#define WAITV(n) asm volatile("s_waitcnt vmcnt(" #n ")" ::: "memory")
template <int N> DEV void waitv() { asm volatile("s_waitcnt vmcnt(%0)" ::"n"(N) : "memory"); }
DEV void wait_lgkm0() { __builtin_amdgcn_s_waitcnt(0xC07F); }
DEV void raw_barrier() {
  asm volatile("" ::: "memory");
  wait_lgkm0();
  __builtin_amdgcn_s_barrier();
  asm volatile("" ::: "memory");
}

template <int BM, int NSEG, int WGM, int WGN, int NSTAGE>
struct GemmCfg {
  static constexpr int NW = WGM * WGN;
  static constexpr int NT_ = NW * 64;
  static constexpr int BN = NSEG * 64;
  static constexpr int WTM = BM / WGM, WTN = BN / WGN;
  static constexpr int MB = WTM / 16, NB = WTN / 16;
  static constexpr int APIECES = BM / 8, BPIECES = NSEG * 8;
  static constexpr int PPW = (APIECES + BPIECES) / NW;
  static constexpr int STAGE_BYTES = (APIECES + BPIECES) * 1024;
  static constexpr int LDS_BYTES = STAGE_BYTES * NSTAGE;
  static_assert((APIECES + BPIECES) % NW == 0, "pieces");
};

template <int BM, int NSEG, int WGM, int WGN, int NSTAGE, bool SCALE, typename PRE>
DEV void gemm_mainloop(PRE pre, const float* sinv  , const u16* __restrict__ A, int lda, const u16* __restrict__ B, int ldb,
                       int m0, int colbase0, int segstride, char* smem,
                       f32x4 (&acc)[GemmCfg<BM, NSEG, WGM, WGN, NSTAGE>::MB][GemmCfg<BM, NSEG, WGM, WGN, NSTAGE>::NB]) {
  using C = GemmCfg<BM, NSEG, WGM, WGN, NSTAGE>;
  constexpr int KT = D_ / 64;
  const int tid = threadIdx.x, lane = tid & 63;
  const int wid = __builtin_amdgcn_readfirstlane(tid >> 6);
  const int wm = wid / WGN, wn = wid % WGN;

  const u16* gp[C::PPW];
  int gstep[C::PPW];

#pragma unroll
  for (int i = 0; i < C::PPW; ++i) {
    int p = wid * C::PPW + i;
    if (p < C::APIECES) {
      int row = p * 8 + (lane >> 3), slot = lane & 7;
      int chunk = slot ^ ((row >> 1) & 7);
      gstep[i] = 64;
      gp[i] = A + (long)(m0 + row) * lda + chunk * 8;
    } else {
      int pb = p - C::APIECES;
      int seg = pb >> 3, k = (pb & 7) * 8 + (lane >> 3), slot = lane & 7;
      int f = ((k >> 1) & 1) | (((k >> 3) & 1) << 1);
      int chunk = slot ^ (f << 1);
      int cb = colbase0 + seg * segstride;
      gstep[i] = 64 * ldb;
      gp[i] = B + (long)k * ldb + cb + chunk * 8;
    }
  }
  auto issue_piece = [&](int stage, int i) {
    char* base = smem + stage * C::STAGE_BYTES + wid * C::PPW * 1024;
    __builtin_amdgcn_global_load_lds((const unsigned*)gp[i], (unsigned*)(base + i * 1024), 16, 0, 0);
    gp[i] += gstep[i];
  };

  const int l15 = lane & 15, g = lane >> 4;
  const int a_off = (wm * C::WTM + l15) * 128;
  const int a_sw = (l15 >> 1);
  const int q = (lane & 15) >> 2, p = lane & 3;
  const int fB = ((q >> 1) & 1) | ((g & 1) << 1);
  const int b_base = C::APIECES * 1024 + (8 * g + q) * 128 + (p & 1) * 8;

#pragma unroll
  for (int s = 0; s < NSTAGE; ++s)
#pragma unroll
    for (int i = 0; i < C::PPW; ++i) issue_piece(s, i);
  pre();

  bf16x8 af[2][C::MB];
  s16x4 bl[2][C::NB][2];
  constexpr int NM = C::NB * C::MB;
  constexpr int NRD = C::NB * 2 + C::MB;
  auto read1 = [&](int stage, int ks, int set, int k) {
    const char* sb = smem + stage * C::STAGE_BYTES;
    if (k < C::NB * 2) {
      int nb = k >> 1, hf = k & 1;
      int gnb = wn * C::NB + nb;
      int seg = gnb >> 2, c = gnb & 3;
      const char* bp = sb + b_base + seg * 8192 + ks * 4096 + ((((2 * c + (p >> 1)) ^ (fB << 1))) << 4);
      bl[set][nb][hf] = tr_read(bp + hf * 512);
    } else {
      int mb = k - C::NB * 2;
      af[set][mb] = *(const bf16x8*)(sb + a_off + mb * 2048 + (((ks * 4 + g) ^ a_sw) << 4));
    }
  };
  auto phase = [&](int set, bool rd, int rstage, int rks, bool iss, int dstage, int head) {
    float scl[C::MB];
    f32x4 t4[C::MB][C::NB];
    if (SCALE) {
      asm volatile("" ::: "memory");
#pragma unroll
      for (int mb = 0; mb < C::MB; ++mb) scl[mb] = sinv[(wm * C::WTM + mb * 16 + l15) * 33 + head];
    }
    constexpr int NSIDE_MAX = NRD + C::PPW;
    constexpr int PER = (NSIDE_MAX + NM - 1) / NM;
    const int nrd = rd ? NRD : 0;
    const int nside = nrd + (iss ? C::PPW : 0);
#pragma unroll
    for (int nb = 0; nb < C::NB; ++nb)
#pragma unroll
      for (int mb = 0; mb < C::MB; ++mb) {
        const int idx = nb * C::MB + mb;
        if (SCALE) {
          t4[mb][nb] = __builtin_amdgcn_mfma_f32_16x16x32_bf16(cat8(bl[set][nb][0], bl[set][nb][1]), af[set][mb], (f32x4){0, 0, 0, 0}, 0, 0, 0);
        } else {
          acc[mb][nb] = __builtin_amdgcn_mfma_f32_16x16x32_bf16(cat8(bl[set][nb][0], bl[set][nb][1]), af[set][mb], acc[mb][nb], 0, 0, 0);
        }
        __builtin_amdgcn_sched_barrier(0);
#pragma unroll
        for (int e = 0; e < PER; ++e) {
          const int k = idx * PER + e;
          if (k < nrd) read1(rstage, rks, set ^ 1, k);
          else if (k < nside) issue_piece(dstage, k - nrd);
        }
        __builtin_amdgcn_sched_barrier(0);
      }
    if (SCALE) {
#pragma unroll
      for (int nb = 0; nb < C::NB; ++nb)
#pragma unroll
        for (int mb = 0; mb < C::MB; ++mb) {
#pragma unroll
          for (int r = 0; r < 4; ++r) acc[mb][nb][r] = fmaf(t4[mb][nb][r], scl[mb], acc[mb][nb][r]);
          asm volatile("" : "+v"(acc[mb][nb]));
        }
    }
  };

  waitv<C::PPW*(NSTAGE - 1)>();
  raw_barrier();
#pragma unroll
  for (int k = 0; k < NRD; ++k) read1(0, 0, 0, k);
  auto step = [&](int cs, bool issue_next, bool more, bool deep) {
    const int ns = (cs + 1) % NSTAGE;
    __builtin_amdgcn_sched_barrier(0);
    phase(0, true, cs, 1, false, 0, 0);
    if (more) {
      if (deep) waitv<C::PPW*(NSTAGE - 2)>(); else waitv<0>();
      raw_barrier();
      __builtin_amdgcn_sched_barrier(0);
      phase(1, true, ns, 0, issue_next, cs, 0);
    } else {
      phase(1, false, 0, 0, false, 0, 0);
    }
  };
  constexpr int NROLL = ((KT - NSTAGE) / NSTAGE);
#pragma unroll 1
  for (int c = 0; c < NROLL; ++c) {
#pragma unroll
    for (int u = 0; u < NSTAGE; ++u) step(u, true, true, true);
  }
#pragma unroll
  for (int kt = NROLL * NSTAGE; kt < KT; ++kt)
    step(kt % NSTAGE, kt + NSTAGE < KT, kt + 1 < KT, kt + NSTAGE - 1 < KT);
  __builtin_amdgcn_sched_barrier(0);
  raw_barrier();
}

__global__ __launch_bounds__(256) void k_init(const u16* __restrict__ x, float2* __restrict__ stats,
                                              const u16* __restrict__ wA, const u16* __restrict__ wB,
                                              float* __restrict__ csA, float* __restrict__ csB,
                                              const u16* __restrict__ woA, const u16* __restrict__ woB, unsigned* __restrict__ junk) {
  const int tid = threadIdx.x;
  if (blockIdx.x >= S_ / 4 + 2 * 12 * CSP) {
    int b = blockIdx.x - (S_ / 4 + 2 * 12 * CSP);
    const u16* w = (b & 32) ? woB : woA;
    const u32x4* src = (const u32x4*)(w + (long)(b & 31) * 32768) + tid;
    u32x4 v[16];
#pragma unroll
    for (int i = 0; i < 16; ++i) v[i] = src[i * 256];
    unsigned a = 0;
#pragma unroll
    for (int i = 0; i < 16; ++i) a ^= v[i][0] ^ v[i][1] ^ v[i][2] ^ v[i][3];
    junk[b * 256 + tid] = a;
    return;
  }
  if (blockIdx.x < S_ / 4) {
    int row = blockIdx.x * 4 + (tid >> 6), lane = tid & 63;
    const bf16x8* px = (const bf16x8*)(x + (long)row * D_ + lane * 16);
    bf16x8 v0 = px[0], v1 = px[1];
    float f[16];
#pragma unroll
    for (int j = 0; j < 8; ++j) { f[j] = bf2f((u16)v0[j]); f[8 + j] = bf2f((u16)v1[j]); }
    float s = 0;
#pragma unroll
    for (int j = 0; j < 16; ++j) s += f[j];
    s += __shfl_xor(s, 1); s += __shfl_xor(s, 2); s += __shfl_xor(s, 4);
    float mu = s * (1.f / 128.f), m2 = 0;
#pragma unroll
    for (int j = 0; j < 16; ++j) { float d = f[j] - mu; m2 += d * d; }
    m2 += __shfl_xor(m2, 1); m2 += __shfl_xor(m2, 2); m2 += __shfl_xor(m2, 4);
    if ((lane & 7) == 0) stats[row * NPART + (lane >> 3)] = make_float2(s, m2);
  } else {
    int b = blockIdx.x - S_ / 4;
    int wsel = b / (12 * CSP); b %= 12 * CSP;
    int cb = b / CSP, rs = b % CSP;
    const u16* w = wsel ? wB : wA;
    float* cs = wsel ? csB : csA;
    __shared__ float red[8][256];
    int cg = tid & 31, rg = tid >> 5;
    float a[8] = {0, 0, 0, 0, 0, 0, 0, 0};
    bf16x8 v[16];
#pragma unroll
    for (int i = 0; i < 16; ++i) v[i] = *(const bf16x8*)(w + (long)(rs * 128 + rg + i * 8) * N3 + cb * 256 + cg * 8);
#pragma unroll
    for (int i = 0; i < 16; ++i)
#pragma unroll
      for (int j = 0; j < 8; ++j) a[j] += bf2f((u16)v[i][j]);
#pragma unroll
    for (int j = 0; j < 8; ++j) red[rg][cg * 8 + j] = a[j];
    __syncthreads();
    float t = 0;
#pragma unroll
    for (int i = 0; i < 8; ++i) t += red[i][tid];
    cs[rs * N3 + cb * 256 + tid] = t;
  }
}

using G1C = GemmCfg<128, 3, 2, 4, 3>;
constexpr int G1_TP = 200;

template <int MODE>
__global__ __launch_bounds__(512, 2) void k_qkv(const u16* __restrict__ x, const u16* __restrict__ w,
                                                const float2* __restrict__ stats, const float* __restrict__ csp,
                                                u16* __restrict__ out  , float* __restrict__ kvp,
                                                float* __restrict__ zp) {
  __shared__ __attribute__((aligned(16))) char smem[G1C::LDS_BYTES + 2048];
  float* s_mean = (float*)(smem + G1C::LDS_BYTES);
  float* s_rstd = s_mean + 128;
  float* s_cs = s_rstd + 128;
  const int tid = threadIdx.x, lane = tid & 63;
  int bid = blockIdx.x;
  int xcd = bid & 7, idx = bid >> 3;
  int mblk = (xcd & 1) * 8 + (idx & 7), nblk = (xcd >> 1) * 4 + (idx >> 3);
  const int m0 = mblk * 128;

  f32x4 acc[G1C::MB][G1C::NB];
#pragma unroll
  for (int i = 0; i < G1C::MB; ++i)
#pragma unroll
    for (int j = 0; j < G1C::NB; ++j) acc[i][j] = (f32x4){0, 0, 0, 0};

  auto pre = [&]() {
  if (tid < 128) {
    const float2* sp = stats + (long)(m0 + tid) * NPART;
    float2 pr[NPART];
#pragma unroll
    for (int i = 0; i < NPART; ++i) pr[i] = sp[i];
    float s = 0;
#pragma unroll
    for (int i = 0; i < NPART; ++i) s += pr[i].x;
    float mean = s * (1.f / D_), m2 = 0;
#pragma unroll
    for (int i = 0; i < NPART; ++i) { float d = pr[i].x * (1.f / 128.f) - mean; m2 += pr[i].y + 128.f * d * d; }
    s_mean[tid] = mean;
    s_rstd[tid] = rsqrtf(m2 * (1.f / D_) + 1e-5f);
  } else if (tid < 128 + 192) {
    int c = tid - 128;
    int col = (c >> 6) * D_ + nblk * 64 + (c & 63);
    float t = 0;
#pragma unroll
    for (int i = 0; i < CSP; ++i) t += csp[i * N3 + col];
    s_cs[c] = t;
  }

  };
  gemm_mainloop<128, 3, 2, 4, 3, false>(pre, nullptr, x, D_, w, N3, m0, nblk * 64, D_, smem, acc);

  const int wid = tid >> 6, wm = wid / 4, wn = wid % 4;
  u16* T = (u16*)smem;
  const int l15 = lane & 15, g = lane >> 4;
#pragma unroll
  for (int mb = 0; mb < G1C::MB; ++mb) {
    const int row = wm * G1C::WTM + mb * 16 + l15;
    const float mean = s_mean[row], rstd = s_rstd[row];
#pragma unroll
    for (int nb = 0; nb < G1C::NB; ++nb) {
      const int col = wn * G1C::WTN + nb * 16 + g * 4;
      const f32x4 csv = *(const f32x4*)(s_cs + col);
      const int seg = col >> 6;
      float y[4];
#pragma unroll
      for (int r = 0; r < 4; ++r) {
        y[r] = (acc[mb][nb][r] - mean * csv[r]) * rstd;
        if (MODE == 0 && seg < 2) y[r] = phi_f(bfr(y[r]));
      }
      u32x2 w2;
      w2[0] = pack2(y[0], y[1]); w2[1] = pack2(y[2], y[3]);
      *(u32x2*)(T + row * G1_TP + col) = w2;
    }
  }
  __syncthreads();
  if (MODE == 1) {
#pragma unroll
    for (int i = 0; i < 6; ++i) {
      const int row = tid >> 2, part = tid & 3;
      u32x4 v = *(const u32x4*)(T + row * G1_TP + i * 32 + part * 8);
      st16(out + ((long)((i >> 1) * H_ + nblk * 2 + (i & 1)) * S_ + m0 + row) * DH + part * 8, v);
    }
  } else {
#pragma unroll
    for (int i = 0; i < 2; ++i) {
      int ch = tid + i * 512;
      int row = ch >> 3, c = ch & 7;
      u32x4 v = *(const u32x4*)(T + row * G1_TP + c * 8);
      st16(out + (long)(m0 + row) * D_ + nblk * 64 + c * 8, v);
    }
    int hh = wid >> 2, dt = (wid >> 1) & 1, et = wid & 1;
    int q = (lane & 15) >> 2, p = lane & 3;
    f32x4 kv = {0, 0, 0, 0}, zz = {0, 0, 0, 0};
    bf16x8 ones;
#pragma unroll
    for (int j = 0; j < 8; ++j) ones[j] = (short)0x3F80;
#pragma unroll
    for (int ks = 0; ks < 4; ++ks) {
      int r0 = ks * 32 + 8 * g + q;
      const char* pa = (const char*)(T + r0 * G1_TP + 64 + hh * 32 + dt * 16 + 4 * p);
      const char* pb = (const char*)(T + r0 * G1_TP + 128 + hh * 32 + et * 16 + 4 * p);
      bf16x8 a = cat8(tr_read(pa), tr_read(pa + 4 * G1_TP * 2));
      bf16x8 b = cat8(tr_read(pb), tr_read(pb + 4 * G1_TP * 2));
      kv = __builtin_amdgcn_mfma_f32_16x16x32_bf16(a, b, kv, 0, 0, 0);
      zz = __builtin_amdgcn_mfma_f32_16x16x32_bf16(a, ones, zz, 0, 0, 0);
    }
    int head = nblk * 2 + hh;
    float* kp = kvp + ((long)(mblk * H_ + head) * 32) * 32;
#pragma unroll
    for (int r = 0; r < 4; ++r) {
      int d = dt * 16 + g * 4 + r;
      kp[d * 32 + et * 16 + l15] = kv[r];
      if (et == 0 && l15 == 0) zp[mblk * D_ + head * 32 + d] = zz[r];
    }
  }
}

__global__ __launch_bounds__(256) void k_lin_mid(const float* __restrict__ kvp, const float* __restrict__ zp,
                                                 const u16* __restrict__ wo, u16* __restrict__ M,
                                                 u16* __restrict__ pq  ) {
  constexpr int WP = 264;
  constexpr int KP = 40;
  __shared__ __attribute__((aligned(16))) u16 sw[32 * WP];
  __shared__ __attribute__((aligned(16))) u16 skv[32 * KP];
  const int tid = threadIdx.x, lane = tid & 63, wid = tid >> 6;
  if (blockIdx.x >= 128) {
    const int r0 = (blockIdx.x - 128) * 8 + wid * 2;
    float* zs = (float*)sw;
    f32x4 zpr[16];
#pragma unroll
    for (int mb = 0; mb < 16; ++mb) zpr[mb] = *(const f32x4*)(zp + mb * D_ + tid * 4);
    bf16x8 v[2][2];
#pragma unroll
    for (int r = 0; r < 2; ++r) {
      const u16* pp = pq + (long)(r0 + r) * D_ + lane * 16;
      v[r][0] = *(const bf16x8*)(pp);
      v[r][1] = *(const bf16x8*)(pp + 8);
    }
    f32x4 zs4 = {0, 0, 0, 0};
#pragma unroll
    for (int mb = 0; mb < 16; ++mb) zs4 += zpr[mb];
    *(f32x4*)(zs + tid * 4) = zs4;
    __syncthreads();
    f32x4 z4[4];
#pragma unroll
    for (int i = 0; i < 4; ++i) z4[i] = *(const f32x4*)(zs + lane * 16 + i * 4);
#pragma unroll
    for (int r = 0; r < 2; ++r) {
      float den = 0;
#pragma unroll
      for (int i = 0; i < 2; ++i)
#pragma unroll
        for (int j = 0; j < 4; ++j)
          den += bf2f((u16)v[r][i][j]) * z4[2 * i][j] + bf2f((u16)v[r][i][4 + j]) * z4[2 * i + 1][j];
      den += __shfl_xor(den, 1);
      const float iv = 1.f / (den + 1e-6f);
      u16* pp = pq + (long)(r0 + r) * D_ + lane * 16;
#pragma unroll
      for (int i = 0; i < 2; ++i) {
        u32x4 o;
#pragma unroll
        for (int j = 0; j < 4; ++j) o[j] = pack2(bf2f((u16)v[r][i][2 * j]) * iv, bf2f((u16)v[r][i][2 * j + 1]) * iv);
        *(u32x4*)(pp + i * 8) = o;
      }
    }
    return;
  }
  const int h = blockIdx.x >> 2, nq = blockIdx.x & 3;
  f32x4 pr[16];
#pragma unroll
  for (int mb = 0; mb < 16; ++mb) pr[mb] = *(const f32x4*)(kvp + ((long)(mb * H_ + h) * 1024) + tid * 4);
  u32x4 wv[4];
#pragma unroll
  for (int i = 0; i < 4; ++i)
    wv[i] = *(const u32x4*)(wo + (long)(h * 32 + (tid >> 5) + i * 8) * D_ + nq * 256 + (tid & 31) * 8);
  f32x4 s4 = {0, 0, 0, 0};
#pragma unroll
  for (int mb = 0; mb < 16; ++mb) s4 += pr[mb];
  {
    u32x2 w2;
    w2[0] = pack2(s4[0], s4[1]); w2[1] = pack2(s4[2], s4[3]);
    *(u32x2*)(skv + (tid >> 3) * KP + (tid & 7) * 4) = w2;
  }
#pragma unroll
  for (int i = 0; i < 4; ++i) *(u32x4*)(sw + ((tid >> 5) + i * 8) * WP + (tid & 31) * 8) = wv[i];
  __syncthreads();
  const int l15 = lane & 15, g = lane >> 4, q = (lane & 15) >> 2, p = lane & 3;
  bf16x8 kf[2];
#pragma unroll
  for (int db = 0; db < 2; ++db) kf[db] = *(const bf16x8*)(skv + (db * 16 + l15) * KP + g * 8);
#pragma unroll
  for (int nb = 0; nb < 4; ++nb) {
    const int n0 = wid * 64 + nb * 16;
    const char* pa = (const char*)(sw + (8 * g + q) * WP + n0 + 4 * p);
    bf16x8 wf = cat8(tr_read(pa), tr_read(pa + 4 * WP * 2));
#pragma unroll
    for (int db = 0; db < 2; ++db) {
      f32x4 d4 = __builtin_amdgcn_mfma_f32_16x16x32_bf16(wf, kf[db], (f32x4){0, 0, 0, 0}, 0, 0, 0);
      u32x2 w2;
      w2[0] = pack2(d4[0], d4[1]); w2[1] = pack2(d4[2], d4[3]);
      *(u32x2*)(M + (long)(h * 32 + db * 16 + l15) * D_ + nq * 256 + n0 + 4 * g) = w2;
    }
  }
}

DEV int crow(int r, int hi) { return (r & 3) + 8 * (r >> 2) + 4 * hi; }

__global__ __launch_bounds__(256, 2) void k_swa(const u16* __restrict__ qkv, u16* __restrict__ att) {
  __shared__ __attribute__((aligned(16))) char sK[384 * 64];
  __shared__ __attribute__((aligned(16))) char sV[384 * 64];
  const int tid = threadIdx.x, lane = tid & 63, wid = tid >> 6;
  const int h = blockIdx.x & 31, qb = blockIdx.x >> 5;
  const int q0 = qb * 128, kbase = q0 - 256;
#pragma unroll
  for (int i = 0; i < 6; ++i) {
    int ch = tid + i * 256;
    int r = ch >> 2, c = ch & 3;
    int key = kbase + r;
    u32x4 kv4 = {0, 0, 0, 0}, vv4 = {0, 0, 0, 0};
    if (key >= 0) {
      kv4 = *(const u32x4*)(qkv + ((long)(H_ + h) * S_ + key) * DH + c * 8);
      vv4 = *(const u32x4*)(qkv + ((long)(2 * H_ + h) * S_ + key) * DH + c * 8);
    }
    *(u32x4*)(sK + r * 64 + ((c ^ ((r >> 2) & 3)) << 4)) = kv4;
    *(u32x4*)(sV + r * 64 + (c << 4)) =vv4;
  }
  const int l31 = lane & 31, hi = lane >> 5;
  const int qw = q0 + wid * 32;
  const int qi = qw + l31;
  bf16x8 qf[2];
#pragma unroll
  for (int ks = 0; ks < 2; ++ks) qf[ks] = *(const bf16x8*)(qkv + ((long)h * S_ + qi) * DH + ks * 16 + hi * 8);
  __syncthreads();

  typedef __attribute__((ext_vector_type(2))) float f32x2;
  const int kt0 = (qw >= 256) ? 0 : (256 - qw) / 32;
  f32x16 sc[9];
  const float scale_l2 = 0.17677669529663687f * 1.4426950408889634f;
  float mx = -3.0e38f;
  const f32x16 zero16 = {0, 0, 0, 0, 0, 0, 0, 0, 0, 0, 0, 0, 0, 0, 0, 0};
#pragma unroll
  for (int kt = 0; kt < 9; ++kt) {
    f32x16 a16;
    if (kt >= kt0) {
      int r = wid * 32 + kt * 32 + l31;
      bf16x8 kf0 = *(const bf16x8*)(sK + r * 64 + ((hi ^ ((r >> 2) & 3)) << 4));
      bf16x8 kf1 = *(const bf16x8*)(sK + r * 64 + (((2 + hi) ^ ((r >> 2) & 3)) << 4));
      a16 = __builtin_amdgcn_mfma_f32_32x32x16_bf16(kf0, qf[0], zero16, 0, 0, 0);
      a16 = __builtin_amdgcn_mfma_f32_32x32x16_bf16(kf1, qf[1], a16, 0, 0, 0);
      if (kt == 0) {
#pragma unroll
        for (int r2 = 0; r2 < 16; ++r2) a16[r2] = (crow(r2, hi) > l31) ? a16[r2] : -3.0e38f;
      }
      if (kt == 8) {
#pragma unroll
        for (int r2 = 0; r2 < 16; ++r2) a16[r2] = (crow(r2, hi) <= l31) ? a16[r2] : -3.0e38f;
      }
#pragma unroll
      for (int r2 = 0; r2 < 16; r2 += 2) mx = fmaxf(fmaxf(mx, a16[r2]), a16[r2 + 1]);
    } else {
#pragma unroll
      for (int i = 0; i < 16; ++i) a16[i] = -3.0e38f;
    }
    sc[kt] = a16;
  }
  mx = fmaxf(mx, __shfl_xor(mx, 32));
  const float mxs = -mx * scale_l2;
  const f32x2 sc2 = {scale_l2, scale_l2}, mx2 = {mxs, mxs};
  f32x2 sum2 = {0, 0};
#pragma unroll
  for (int kt = 0; kt < 9; ++kt)
#pragma unroll
    for (int r = 0; r < 16; r += 2) {
      f32x2 t = {sc[kt][r], sc[kt][r + 1]};
      t = t * sc2 + mx2;
      f32x2 e = {__builtin_amdgcn_exp2f(t[0]), __builtin_amdgcn_exp2f(t[1])};
      sc[kt][r] = e[0]; sc[kt][r + 1] = e[1];
      sum2 += e;
    }
  float sum = sum2[0] + sum2[1];
  sum += __shfl_xor(sum, 32);
  f32x16 o = zero16;
  const int q4 = (lane & 15) >> 2, p4 = lane & 3, blk = (lane >> 4) & 1;
#pragma unroll
  for (int kt = 0; kt < 9; ++kt) {
#pragma unroll
    for (int s = 0; s < 2; ++s) {
      u32x4 pk;
#pragma unroll
      for (int j = 0; j < 4; ++j) pk[j] = pack2(sc[kt][8 * s + 2 * j], sc[kt][8 * s + 2 * j + 1]);
      bf16x8 pf = __builtin_bit_cast(bf16x8, pk);
      int r0 = wid * 32 + kt * 32 + 16 * s + 4 * hi + q4;
      const char* vp = sV + r0 * 64 + (16 * blk + 4 * p4) * 2;
      bf16x8 vf = cat8(tr_read(vp), tr_read(vp + 8 * 64));
      o = __builtin_amdgcn_mfma_f32_32x32x16_bf16(vf, pf, o, 0, 0, 0);
    }
  }
  float inv = 1.f / sum;
  u16* op = att + (long)qi * D_ + h * 32;
#pragma unroll
  for (int gq = 0; gq < 4; ++gq) {
    u32x2 w2;
    w2[0] = pack2(o[4 * gq + 0] * inv, o[4 * gq + 1] * inv);
    w2[1] = pack2(o[4 * gq + 2] * inv, o[4 * gq + 3] * inv);
    *(u32x2*)(op + 8 * gq + 4 * hi) = w2;
  }
}

using G2C = GemmCfg<64, 2, 2, 4, 4>;

template <bool LIN>
__global__ __launch_bounds__(512, 2) void k_out(const u16* __restrict__ a, const u16* __restrict__ w,
                                                const u16* __restrict__ xold, u16* __restrict__ xnew,
                                                float2* __restrict__ stats, const float* __restrict__ zsum) {
  __shared__ __attribute__((aligned(16))) char smem[G2C::LDS_BYTES + 64 * 33 * 4];
  float* sinv = (float*)(smem + G2C::LDS_BYTES);
  const int tid = threadIdx.x, lane = tid & 63;
  int bid = blockIdx.x;
  int xcd = bid & 7, idx = bid >> 3;
  int mblk = xcd * 4 + (idx & 3), nblk = idx >> 2;
  const int m0 = mblk * 64;
  f32x4 acc[G2C::MB][G2C::NB];
#pragma unroll
  for (int i = 0; i < G2C::MB; ++i)
#pragma unroll
    for (int j = 0; j < G2C::NB; ++j) acc[i][j] = (f32x4){0, 0, 0, 0};
  auto pre = [&]() {
    if (LIN) {
      f32x4 v4 = *(const f32x4*)(zsum + (long)m0 * 32 + tid * 4);
      float* dst = sinv + (tid >> 3) * 33 + (tid & 7) * 4;
#pragma unroll
      for (int j = 0; j < 4; ++j) dst[j] = v4[j];
    }
  };
  gemm_mainloop<64, 2, 2, 4, 4, LIN>(pre, sinv, a, D_, w, D_, m0, nblk * 128, 64, smem, acc);

  const int wid = tid >> 6, wm = wid / 4, wn = wid % 4;
  float* T = (float*)smem;
  constexpr int TP = 132;
  const int l15 = lane & 15, g = lane >> 4;
#pragma unroll
  for (int mb = 0; mb < G2C::MB; ++mb)
#pragma unroll
    for (int nb = 0; nb < G2C::NB; ++nb)
      *(f32x4*)(T + (wm * 32 + mb * 16 + l15) * TP + wn * 32 + nb * 16 + g * 4) = acc[mb][nb];
  __syncthreads();
  int row = tid >> 3, part = tid & 7;
  const u16* xo = xold + (long)(m0 + row) * D_ + nblk * 128 + part * 16;
  u16* xn = xnew + (long)(m0 + row) * D_ + nblk * 128 + part * 16;
  float v[16];
  float s = 0;
#pragma unroll
  for (int i = 0; i < 2; ++i) {
    bf16x8 xv = *(const bf16x8*)(xo + i * 8);
    u32x4 o;
#pragma unroll
    for (int j = 0; j < 4; ++j) {
      float f0 = bfr(bf2f((u16)xv[2 * j]) + T[row * TP + part * 16 + i * 8 + 2 * j]);
      float f1 = bfr(bf2f((u16)xv[2 * j + 1]) + T[row * TP + part * 16 + i * 8 + 2 * j + 1]);
      v[i * 8 + 2 * j] = f0; v[i * 8 + 2 * j + 1] = f1;
      s += f0 + f1;
      o[j] = pack2(f0, f1);
    }
    *(u32x4*)(xn + i * 8) = o;
  }
  s += __shfl_xor(s, 1); s += __shfl_xor(s, 2); s += __shfl_xor(s, 4);
  float mu = s * (1.f / 128.f), m2 = 0;
#pragma unroll
  for (int j = 0; j < 16; ++j) { float d = v[j] - mu; m2 += d * d; }
  m2 += __shfl_xor(m2, 1); m2 += __shfl_xor(m2, 2); m2 += __shfl_xor(m2, 4);
  if (part == 0) stats[(long)(m0 + row) * NPART + nblk] = make_float2(s, m2);
}

extern "C" void kernel_launch(void* const* d_in, const int* in_sizes, int n_in, void* d_out, int out_size,
                              void* d_ws, size_t ws_size, hipStream_t stream) {
  const u16* x_in = (const u16*)d_in[0];
  const u16* A_wqkv = (const u16*)d_in[1];
  const u16* A_wo = (const u16*)d_in[2];
  const u16* B_wqkv = (const u16*)d_in[4];
  const u16* B_wo = (const u16*)d_in[5];
  u16* out = (u16*)d_out;
  char* ws = (char*)d_ws;
  size_t off = 0;
  auto take = [&](size_t bytes) { char* p = ws + off; off += (bytes + 255) & ~(size_t)255; return p; };
  u16* xb0 = (u16*)take((size_t)S_ * D_ * 2);
  u16* xb1 = (u16*)take((size_t)S_ * D_ * 2);
  u16* qkv = (u16*)take((size_t)S_ * N3 * 2);
  u16* att = (u16*)take((size_t)S_ * D_ * 2);
  u16* Mm = (u16*)take((size_t)D_ * D_ * 2);
  float* kvp = (float*)take((size_t)16 * H_ * 1024 * 4);
  float* zp = (float*)take((size_t)16 * D_ * 4);
  float2* stats = (float2*)take((size_t)S_ * NPART * 8);
  float* csA = (float*)take((size_t)CSP * N3 * 4);
  float* csB = (float*)take((size_t)CSP * N3 * 4);
  float* zsum = (float*)take((size_t)S_ * 32 * 4);

  unsigned* junk = (unsigned*)take((size_t)64 * 256 * 4);
  k_init<<<S_ / 4 + 2 * 12 * CSP + 64, 256, 0, stream>>>(x_in, stats, A_wqkv, B_wqkv, csA, csB, A_wo, B_wo, junk);
  k_qkv<0><<<256, 512, 0, stream>>>(x_in, A_wqkv, stats, csA, att, kvp, zp);
  k_lin_mid<<<384, 256, 0, stream>>>(kvp, zp, A_wo, Mm, att);
  k_out<false><<<256, 512, 0, stream>>>(att, Mm, x_in, xb0, stats, zsum);
  k_qkv<1><<<256, 512, 0, stream>>>(xb0, B_wqkv, stats, csB, qkv, kvp, zp);
  k_swa<<<512, 256, 0, stream>>>(qkv, att);
  k_out<false><<<256, 512, 0, stream>>>(att, B_wo, xb0, xb1, stats, zsum);
  k_qkv<0><<<256, 512, 0, stream>>>(xb1, A_wqkv, stats, csA, att, kvp, zp);
  k_lin_mid<<<384, 256, 0, stream>>>(kvp, zp, A_wo, Mm, att);
  k_out<false><<<256, 512, 0, stream>>>(att, Mm, xb1, xb0, stats, zsum);
  k_qkv<1><<<256, 512, 0, stream>>>(xb0, B_wqkv, stats, csB, qkv, kvp, zp);
  k_swa<<<512, 256, 0, stream>>>(qkv, att);
  k_out<false><<<256, 512, 0, stream>>>(att, B_wo, xb0, out, stats, zsum);
}
```

```cpp
#include <hip/hip_runtime.h>
#include <hip/hip_bf16.h>
#include <stdint.h>
#include <type_traits>

constexpr int S_ = 2048, D_ = 1024, H_ = 32, DH = 32, WIN = 256;
constexpr int N3 = 3 * D_;
constexpr int NPART = 8;
constexpr int CSP = 8;

typedef __attribute__((ext_vector_type(8))) short bf16x8;
typedef __attribute__((ext_vector_type(4))) short s16x4;
typedef __attribute__((ext_vector_type(4))) float f32x4;
typedef __attribute__((ext_vector_type(16))) float f32x16;
typedef __attribute__((ext_vector_type(4))) unsigned u32x4;
typedef __attribute__((ext_vector_type(2))) unsigned u32x2;
typedef __attribute__((address_space(3))) s16x4* lds_s16x4_p;
typedef unsigned short u16;

#define DEV __device__ __forceinline__

DEV float bf2f(u16 v) { return __uint_as_float(((unsigned)v) << 16); }
typedef __attribute__((ext_vector_type(2))) float f32x2_t;
typedef __attribute__((ext_vector_type(2))) __bf16 bf16x2_t;
DEV unsigned pack2(float lo, float hi) {
  f32x2_t v = {lo, hi};
  bf16x2_t b = __builtin_convertvector(v, bf16x2_t);
  return __builtin_bit_cast(unsigned, b);
}
DEV u16 f2bf(float f) { return (u16)(pack2(f, f) & 0xffffu); }
DEV float bfr(float f) { return __uint_as_float(pack2(f, f) << 16); }
DEV float phi_f(float t) { return (t > 0.f ? t + 1.f : __expf(t)) + 1e-6f; }

DEV void st16(void* p, u32x4 v) {
  asm volatile("global_store_dwordx4 %0, %1, off sc1\n\ts_nop 1" ::"v"(p), "v"(v) : "memory");
}
DEV s16x4 tr_read(const char* p) {
  return __builtin_amdgcn_ds_read_tr16_b64_v4i16((lds_s16x4_p)(p));
}
DEV bf16x8 cat8(s16x4 a, s16x4 b) { return __builtin_shufflevector(a, b, 0, 1, 2, 3, 4, 5, 6, 7); }

template <int N> DEV void waitv() { asm volatile("s_waitcnt vmcnt(%0)" ::"n"(N) : "memory"); }
DEV void wait_lgkm0() { __builtin_amdgcn_s_waitcnt(0xC07F); }
DEV void raw_barrier() {
  asm volatile("" ::: "memory");
  wait_lgkm0();
  __builtin_amdgcn_s_barrier();
  asm volatile("" ::: "memory");
}

template <int BM, int NSEG, int WGM, int WGN, int NSTAGE>
struct GemmCfg {
  static constexpr int NW = WGM * WGN;
  static constexpr int NT_ = NW * 64;
  static constexpr int BN = NSEG * 64;
  static constexpr int WTM = BM / WGM, WTN = BN / WGN;
  static constexpr int MB = WTM / 16, NB = WTN / 16;
  static constexpr int APIECES = BM / 8, BPIECES = NSEG * 8;
  static constexpr int PPW = (APIECES + BPIECES) / NW;
  static constexpr int STAGE_BYTES = (APIECES + BPIECES) * 1024;
  static constexpr int LDS_BYTES = STAGE_BYTES * NSTAGE;
  static_assert((APIECES + BPIECES) % NW == 0, "pieces");
};

template <int BM, int NSEG, int WGM, int WGN, int NSTAGE, typename PRE>
DEV void gemm_mainloop(PRE pre, const u16* __restrict__ A, int lda, const u16* __restrict__ B, int ldb,
                       int m0, int colbase0, int segstride, char* smem,
                       f32x4 (&acc)[GemmCfg<BM, NSEG, WGM, WGN, NSTAGE>::MB][GemmCfg<BM, NSEG, WGM, WGN, NSTAGE>::NB]) {
  using C = GemmCfg<BM, NSEG, WGM, WGN, NSTAGE>;
  constexpr int KT = D_ / 64;
  const int tid = threadIdx.x, lane = tid & 63;
  const int wid = __builtin_amdgcn_readfirstlane(tid >> 6);
  const int wm = wid / WGN, wn = wid % WGN;

  const u16* gp[C::PPW];
  int gstep[C::PPW];

#pragma unroll
  for (int i = 0; i < C::PPW; ++i) {
    int p = wid * C::PPW + i;
    if (p < C::APIECES) {
      int row = p * 8 + (lane >> 3), slot = lane & 7;
      int chunk = slot ^ ((row >> 1) & 7);
      gstep[i] = 64;
      gp[i] = A + (long)(m0 + row) * lda + chunk * 8;
    } else {
      int pb = p - C::APIECES;
      int seg = pb >> 3, k = (pb & 7) * 8 + (lane >> 3), slot = lane & 7;
      int f = ((k >> 1) & 1) | (((k >> 3) & 1) << 1);
      int chunk = slot ^ (f << 1);
      int cb = colbase0 + seg * segstride;
      gstep[i] = 64 * ldb;
      gp[i] = B + (long)k * ldb + cb + chunk * 8;
    }
  }
  auto issue_piece = [&](int stage, int i) {
    char* base = smem + stage * C::STAGE_BYTES + wid * C::PPW * 1024;
    __builtin_amdgcn_global_load_lds((const unsigned*)gp[i], (unsigned*)(base + i * 1024), 16, 0, 0);
    gp[i] += gstep[i];
  };

  const int l15 = lane & 15, g = lane >> 4;
  const int a_off = (wm * C::WTM + l15) * 128;
  const int a_sw = (l15 >> 1);
  const int q = (lane & 15) >> 2, p = lane & 3;
  const int fB = ((q >> 1) & 1) | ((g & 1) << 1);
  const int b_base = C::APIECES * 1024 + (8 * g + q) * 128 + (p & 1) * 8;

#pragma unroll
  for (int s = 0; s < NSTAGE; ++s)
#pragma unroll
    for (int i = 0; i < C::PPW; ++i) issue_piece(s, i);
  pre();

  bf16x8 af[2][C::MB];
  s16x4 bl[2][C::NB][2];
  constexpr int NM = C::NB * C::MB;
  constexpr int NRD = C::NB * 2 + C::MB;
  auto read1 = [&](int stage, int ks, int set, int k) {
    const char* sb = smem + stage * C::STAGE_BYTES;
    if (k < C::NB * 2) {
      int nb = k >> 1, hf = k & 1;
      int gnb = wn * C::NB + nb;
      int seg = gnb >> 2, c = gnb & 3;
      const char* bp = sb + b_base + seg * 8192 + ks * 4096 + ((((2 * c + (p >> 1)) ^ (fB << 1))) << 4);
      bl[set][nb][hf] = tr_read(bp + hf * 512);
    } else {
      int mb = k - C::NB * 2;
      af[set][mb] = *(const bf16x8*)(sb + a_off + mb * 2048 + (((ks * 4 + g) ^ a_sw) << 4));
    }
  };
  auto phase = [&](int set, bool rd, int rstage, int rks, bool iss, int dstage) {
    constexpr int NSIDE_MAX = NRD + C::PPW;
    constexpr int PER = (NSIDE_MAX + NM - 1) / NM;
    const int nrd = rd ? NRD : 0;
    const int nside = nrd + (iss ? C::PPW : 0);
#pragma unroll
    for (int nb = 0; nb < C::NB; ++nb)
#pragma unroll
      for (int mb = 0; mb < C::MB; ++mb) {
        const int idx = nb * C::MB + mb;
        acc[mb][nb] = __builtin_amdgcn_mfma_f32_16x16x32_bf16(cat8(bl[set][nb][0], bl[set][nb][1]), af[set][mb], acc[mb][nb], 0, 0, 0);
        __builtin_amdgcn_sched_barrier(0);
#pragma unroll
        for (int e = 0; e < PER; ++e) {
          const int k = idx * PER + e;
          if (k < nrd) read1(rstage, rks, set ^ 1, k);
          else if (k < nside) issue_piece(dstage, k - nrd);
        }
        __builtin_amdgcn_sched_barrier(0);
      }
  };

  waitv<C::PPW*(NSTAGE - 1)>();
  raw_barrier();
#pragma unroll
  for (int k = 0; k < NRD; ++k) read1(0, 0, 0, k);
  auto step = [&](int cs, bool issue_next, bool more, bool deep) {
    const int ns = (cs + 1) % NSTAGE;
    __builtin_amdgcn_sched_barrier(0);
    phase(0, true, cs, 1, false, 0);
    if (more) {
      if (deep) waitv<C::PPW*(NSTAGE - 2)>(); else waitv<0>();
      raw_barrier();
      __builtin_amdgcn_sched_barrier(0);
      phase(1, true, ns, 0, issue_next, cs);
    } else {
      phase(1, false, 0, 0, false, 0);
    }
  };
  constexpr int NROLL = ((KT - NSTAGE) / NSTAGE);
#pragma unroll 1
  for (int c = 0; c < NROLL; ++c) {
#pragma unroll
    for (int u = 0; u < NSTAGE; ++u) step(u, true, true, true);
  }
#pragma unroll
  for (int kt = NROLL * NSTAGE; kt < KT; ++kt)
    step(kt % NSTAGE, kt + NSTAGE < KT, kt + 1 < KT, kt + NSTAGE - 1 < KT);
  __builtin_amdgcn_sched_barrier(0);
  raw_barrier();
}

__global__ __launch_bounds__(256) void k_init(const u16* __restrict__ x, float2* __restrict__ stats,
                                              const u16* __restrict__ wA, const u16* __restrict__ wB,
                                              float* __restrict__ csA, float* __restrict__ csB,
                                              const u16* __restrict__ woA, const u16* __restrict__ woB, unsigned* __restrict__ junk) {
  const int tid = threadIdx.x;
  if (blockIdx.x >= S_ / 4 + 2 * 12 * CSP) {
    int b = blockIdx.x - (S_ / 4 + 2 * 12 * CSP);
    const u16* w = (b & 32) ? woB : woA;
    const u32x4* src = (const u32x4*)(w + (long)(b & 31) * 32768) + tid;
    u32x4 v[16];
#pragma unroll
    for (int i = 0; i < 16; ++i) v[i] = src[i * 256];
    unsigned a = 0;
#pragma unroll
    for (int i = 0; i < 16; ++i) a ^= v[i][0] ^ v[i][1] ^ v[i][2] ^ v[i][3];
    junk[b * 256 + tid] = a;
    return;
  }
  if (blockIdx.x < S_ / 4) {
    int row = blockIdx.x * 4 + (tid >> 6), lane = tid & 63;
    const bf16x8* px = (const bf16x8*)(x + (long)row * D_ + lane * 16);
    bf16x8 v0 = px[0], v1 = px[1];
    float f[16];
#pragma unroll
    for (int j = 0; j < 8; ++j) { f[j] = bf2f((u16)v0[j]); f[8 + j] = bf2f((u16)v1[j]); }
    float s = 0;
#pragma unroll
    for (int j = 0; j < 16; ++j) s += f[j];
    s += __shfl_xor(s, 1); s += __shfl_xor(s, 2); s += __shfl_xor(s, 4);
    float mu = s * (1.f / 128.f), m2 = 0;
#pragma unroll
    for (int j = 0; j < 16; ++j) { float d = f[j] - mu; m2 += d * d; }
    m2 += __shfl_xor(m2, 1); m2 += __shfl_xor(m2, 2); m2 += __shfl_xor(m2, 4);
    if ((lane & 7) == 0) stats[row * NPART + (lane >> 3)] = make_float2(s, m2);
  } else {
    int b = blockIdx.x - S_ / 4;
    int wsel = b / (12 * CSP); b %= 12 * CSP;
    int cb = b / CSP, rs = b % CSP;
    const u16* w = wsel ? wB : wA;
    float* cs = wsel ? csB : csA;
    __shared__ float red[8][256];
    int cg = tid & 31, rg = tid >> 5;
    float a[8] = {0, 0, 0, 0, 0, 0, 0, 0};
    bf16x8 v[16];
#pragma unroll
    for (int i = 0; i < 16; ++i) v[i] = *(const bf16x8*)(w + (long)(rs * 128 + rg + i * 8) * N3 + cb * 256 + cg * 8);
#pragma unroll
    for (int i = 0; i < 16; ++i)
#pragma unroll
      for (int j = 0; j < 8; ++j) a[j] += bf2f((u16)v[i][j]);
#pragma unroll
    for (int j = 0; j < 8; ++j) red[rg][cg * 8 + j] = a[j];
    __syncthreads();
    float t = 0;
#pragma unroll
    for (int i = 0; i < 8; ++i) t += red[i][tid];
    cs[rs * N3 + cb * 256 + tid] = t;
  }
}

using G1C = GemmCfg<128, 3, 2, 4, 3>;
constexpr int G1_TP = 200;

template <int MODE>
__global__ __launch_bounds__(512, 2) void k_qkv(const u16* __restrict__ x, const u16* __restrict__ w,
                                                const float2* __restrict__ stats, const float* __restrict__ csp,
                                                u16* __restrict__ out  , float* __restrict__ kvp,
                                                float* __restrict__ zp) {
  __shared__ __attribute__((aligned(16))) char smem[G1C::LDS_BYTES + 2048];
  float* s_mean = (float*)(smem + G1C::LDS_BYTES);
  float* s_rstd = s_mean + 128;
  float* s_cs = s_rstd + 128;
  const int tid = threadIdx.x, lane = tid & 63;
  int bid = blockIdx.x;
  int xcd = bid & 7, idx = bid >> 3;
  int mblk = (xcd & 1) * 8 + (idx & 7), nblk = (xcd >> 1) * 4 + (idx >> 3);
  const int m0 = mblk * 128;

  f32x4 acc[G1C::MB][G1C::NB];
#pragma unroll
  for (int i = 0; i < G1C::MB; ++i)
#pragma unroll
    for (int j = 0; j < G1C::NB; ++j) acc[i][j] = (f32x4){0, 0, 0, 0};

  auto pre = [&]() {
  if (tid < 128) {
    const float2* sp = stats + (long)(m0 + tid) * NPART;
    float2 pr[NPART];
#pragma unroll
    for (int i = 0; i < NPART; ++i) pr[i] = sp[i];
    float s = 0;
#pragma unroll
    for (int i = 0; i < NPART; ++i) s += pr[i].x;
    float mean = s * (1.f / D_), m2 = 0;
#pragma unroll
    for (int i = 0; i < NPART; ++i) { float d = pr[i].x * (1.f / 128.f) - mean; m2 += pr[i].y + 128.f * d * d; }
    s_mean[tid] = mean;
    s_rstd[tid] = rsqrtf(m2 * (1.f / D_) + 1e-5f);
  } else if (tid < 128 + 192) {
    int c = tid - 128;
    int col = (c >> 6) * D_ + nblk * 64 + (c & 63);
    float t = 0;
#pragma unroll
    for (int i = 0; i < CSP; ++i) t += csp[i * N3 + col];
    s_cs[c] = t;
  }

  };
  gemm_mainloop<128, 3, 2, 4, 3>(pre, x, D_, w, N3, m0, nblk * 64, D_, smem, acc);

  const int wid = tid >> 6, wm = wid / 4, wn = wid % 4;
  u16* T = (u16*)smem;
  const int l15 = lane & 15, g = lane >> 4;
#pragma unroll
  for (int mb = 0; mb < G1C::MB; ++mb) {
    const int row = wm * G1C::WTM + mb * 16 + l15;
    const float mean = s_mean[row], rstd = s_rstd[row];
#pragma unroll
    for (int nb = 0; nb < G1C::NB; ++nb) {
      const int col = wn * G1C::WTN + nb * 16 + g * 4;
      const f32x4 csv = *(const f32x4*)(s_cs + col);
      const int seg = col >> 6;
      float y[4];
#pragma unroll
      for (int r = 0; r < 4; ++r) {
        y[r] = (acc[mb][nb][r] - mean * csv[r]) * rstd;
        if (MODE == 0 && seg < 2) y[r] = phi_f(bfr(y[r]));
      }
      u32x2 w2;
      w2[0] = pack2(y[0], y[1]); w2[1] = pack2(y[2], y[3]);
      *(u32x2*)(T + row * G1_TP + col) = w2;
    }
  }
  __syncthreads();
  if (MODE == 1) {
#pragma unroll
    for (int i = 0; i < 6; ++i) {
      const int row = tid >> 2, part = tid & 3;
      u32x4 v = *(const u32x4*)(T + row * G1_TP + i * 32 + part * 8);
      st16(out + ((long)((i >> 1) * H_ + nblk * 2 + (i & 1)) * S_ + m0 + row) * DH + part * 8, v);
    }
  } else {
#pragma unroll
    for (int i = 0; i < 2; ++i) {
      int ch = tid + i * 512;
      int row = ch >> 3, c = ch & 7;
      u32x4 v = *(const u32x4*)(T + row * G1_TP + c * 8);
      st16(out + (long)(m0 + row) * D_ + nblk * 64 + c * 8, v);
    }
    int hh = wid >> 2, dt = (wid >> 1) & 1, et = wid & 1;
    int q = (lane & 15) >> 2, p = lane & 3;
    f32x4 kv = {0, 0, 0, 0}, zz = {0, 0, 0, 0};
    bf16x8 ones;
#pragma unroll
    for (int j = 0; j < 8; ++j) ones[j] = (short)0x3F80;
#pragma unroll
    for (int ks = 0; ks < 4; ++ks) {
      int r0 = ks * 32 + 8 * g + q;
      const char* pa = (const char*)(T + r0 * G1_TP + 64 + hh * 32 + dt * 16 + 4 * p);
      const char* pb = (const char*)(T + r0 * G1_TP + 128 + hh * 32 + et * 16 + 4 * p);
      bf16x8 a = cat8(tr_read(pa), tr_read(pa + 4 * G1_TP * 2));
      bf16x8 b = cat8(tr_read(pb), tr_read(pb + 4 * G1_TP * 2));
      kv = __builtin_amdgcn_mfma_f32_16x16x32_bf16(a, b, kv, 0, 0, 0);
      zz = __builtin_amdgcn_mfma_f32_16x16x32_bf16(a, ones, zz, 0, 0, 0);
    }
    int head = nblk * 2 + hh;
    float* kp = kvp + ((long)(mblk * H_ + head) * 32) * 32;
#pragma unroll
    for (int r = 0; r < 4; ++r) {
      int d = dt * 16 + g * 4 + r;
      kp[d * 32 + et * 16 + l15] = kv[r];
      if (et == 0 && l15 == 0) zp[mblk * D_ + head * 32 + d] = zz[r];
    }
  }
}

__global__ __launch_bounds__(256) void k_lin_mid(const float* __restrict__ kvp, const float* __restrict__ zp,
                                                 const u16* __restrict__ wo, u16* __restrict__ M,
                                                 u16* __restrict__ pq  ) {
  constexpr int WP = 264;
  constexpr int KP = 40;
  __shared__ __attribute__((aligned(16))) u16 sw[32 * WP];
  __shared__ __attribute__((aligned(16))) u16 skv[32 * KP];
  const int tid = threadIdx.x, lane = tid & 63, wid = tid >> 6;
  if (blockIdx.x >= 128) {
    const int r0 = (blockIdx.x - 128) * 8 + wid * 2;
    float* zs = (float*)sw;
    f32x4 zpr[16];
#pragma unroll
    for (int mb = 0; mb < 16; ++mb) zpr[mb] = *(const f32x4*)(zp + mb * D_ + tid * 4);
    bf16x8 v[2][2];
#pragma unroll
    for (int r = 0; r < 2; ++r) {
      const u16* pp = pq + (long)(r0 + r) * D_ + lane * 16;
      v[r][0] = *(const bf16x8*)(pp);
      v[r][1] = *(const bf16x8*)(pp + 8);
    }
    f32x4 zs4 = {0, 0, 0, 0};
#pragma unroll
    for (int mb = 0; mb < 16; ++mb) zs4 += zpr[mb];
    *(f32x4*)(zs + tid * 4) = zs4;
    __syncthreads();
    f32x4 z4[4];
#pragma unroll
    for (int i = 0; i < 4; ++i) z4[i] = *(const f32x4*)(zs + lane * 16 + i * 4);
#pragma unroll
    for (int r = 0; r < 2; ++r) {
      float den = 0;
#pragma unroll
      for (int i = 0; i < 2; ++i)
#pragma unroll
        for (int j = 0; j < 4; ++j)
          den += bf2f((u16)v[r][i][j]) * z4[2 * i][j] + bf2f((u16)v[r][i][4 + j]) * z4[2 * i + 1][j];
      den += __shfl_xor(den, 1);
      const float iv = 1.f / (den + 1e-6f);
      u16* pp = pq + (long)(r0 + r) * D_ + lane * 16;
#pragma unroll
      for (int i = 0; i < 2; ++i) {
        u32x4 o;
#pragma unroll
        for (int j = 0; j < 4; ++j) o[j] = pack2(bf2f((u16)v[r][i][2 * j]) * iv, bf2f((u16)v[r][i][2 * j + 1]) * iv);
        *(u32x4*)(pp + i * 8) = o;
      }
    }
    return;
  }
  const int h = blockIdx.x >> 2, nq = blockIdx.x & 3;
  f32x4 pr[16];
#pragma unroll
  for (int mb = 0; mb < 16; ++mb) pr[mb] = *(const f32x4*)(kvp + ((long)(mb * H_ + h) * 1024) + tid * 4);
  u32x4 wv[4];
#pragma unroll
  for (int i = 0; i < 4; ++i)
    wv[i] = *(const u32x4*)(wo + (long)(h * 32 + (tid >> 5) + i * 8) * D_ + nq * 256 + (tid & 31) * 8);
  f32x4 s4 = {0, 0, 0, 0};
#pragma unroll
  for (int mb = 0; mb < 16; ++mb) s4 += pr[mb];
  {
    u32x2 w2;
    w2[0] = pack2(s4[0], s4[1]); w2[1] = pack2(s4[2], s4[3]);
    *(u32x2*)(skv + (tid >> 3) * KP + (tid & 7) * 4) = w2;
  }
#pragma unroll
  for (int i = 0; i < 4; ++i) *(u32x4*)(sw + ((tid >> 5) + i * 8) * WP + (tid & 31) * 8) = wv[i];
  __syncthreads();
  const int l15 = lane & 15, g = lane >> 4, q = (lane & 15) >> 2, p = lane & 3;
  bf16x8 kf[2];
#pragma unroll
  for (int db = 0; db < 2; ++db) kf[db] = *(const bf16x8*)(skv + (db * 16 + l15) * KP + g * 8);
#pragma unroll
  for (int nb = 0; nb < 4; ++nb) {
    const int n0 = wid * 64 + nb * 16;
    const char* pa = (const char*)(sw + (8 * g + q) * WP + n0 + 4 * p);
    bf16x8 wf = cat8(tr_read(pa), tr_read(pa + 4 * WP * 2));
#pragma unroll
    for (int db = 0; db < 2; ++db) {
      f32x4 d4 = __builtin_amdgcn_mfma_f32_16x16x32_bf16(wf, kf[db], (f32x4){0, 0, 0, 0}, 0, 0, 0);
      u32x2 w2;
      w2[0] = pack2(d4[0], d4[1]); w2[1] = pack2(d4[2], d4[3]);
      *(u32x2*)(M + (long)(h * 32 + db * 16 + l15) * D_ + nq * 256 + n0 + 4 * g) = w2;
    }
  }
}

DEV int crow(int r, int hi) { return (r & 3) + 8 * (r >> 2) + 4 * hi; }

__global__ __launch_bounds__(256, 2) void k_swa(const u16* __restrict__ qkv, u16* __restrict__ att) {
  __shared__ __attribute__((aligned(16))) char sK[384 * 64];
  __shared__ __attribute__((aligned(16))) char sV[384 * 64];
  const int tid = threadIdx.x, lane = tid & 63, wid = tid >> 6;
  const int h = blockIdx.x & 31, qb = blockIdx.x >> 5;
  const int q0 = qb * 128, kbase = q0 - 256;
#pragma unroll
  for (int i = 0; i < 6; ++i) {
    int ch = tid + i * 256;
    int r = ch >> 2, c = ch & 3;
    int key = kbase + r;
    u32x4 kv4 = {0, 0, 0, 0}, vv4 = {0, 0, 0, 0};
    if (key >= 0) {
      kv4 = *(const u32x4*)(qkv + ((long)(H_ + h) * S_ + key) * DH + c * 8);
      vv4 = *(const u32x4*)(qkv + ((long)(2 * H_ + h) * S_ + key) * DH + c * 8);
    }
    *(u32x4*)(sK + r * 64 + ((c ^ ((r >> 2) & 3)) << 4)) = kv4;
    *(u32x4*)(sV + r * 64 + (c << 4)) =vv4;
  }
  const int l31 = lane & 31, hi = lane >> 5;
  const int qw = q0 + wid * 32;
  const int qi = qw + l31;
  bf16x8 qf[2];
#pragma unroll
  for (int ks = 0; ks < 2; ++ks) qf[ks] = *(const bf16x8*)(qkv + ((long)h * S_ + qi) * DH + ks * 16 + hi * 8);
  __syncthreads();

  typedef __attribute__((ext_vector_type(2))) float f32x2;
  const int kt0 = (qw >= 256) ? 0 : (256 - qw) / 32;
  f32x16 sc[9];
  const float scale_l2 = 0.17677669529663687f * 1.4426950408889634f;
  float mx = -3.0e38f;
  const f32x16 zero16 = {0, 0, 0, 0, 0, 0, 0, 0, 0, 0, 0, 0, 0, 0, 0, 0};
#pragma unroll
  for (int kt = 0; kt < 9; ++kt) {
    f32x16 a16;
    if (kt >= kt0) {
      int r = wid * 32 + kt * 32 + l31;
      bf16x8 kf0 = *(const bf16x8*)(sK + r * 64 + ((hi ^ ((r >> 2) & 3)) << 4));
      bf16x8 kf1 = *(const bf16x8*)(sK + r * 64 + (((2 + hi) ^ ((r >> 2) & 3)) << 4));
      a16 = __builtin_amdgcn_mfma_f32_32x32x16_bf16(kf0, qf[0], zero16, 0, 0, 0);
      a16 = __builtin_amdgcn_mfma_f32_32x32x16_bf16(kf1, qf[1], a16, 0, 0, 0);
      if (kt == 0) {
#pragma unroll
        for (int r2 = 0; r2 < 16; ++r2) a16[r2] = (crow(r2, hi) > l31) ? a16[r2] : -3.0e38f;
      }
      if (kt == 8) {
#pragma unroll
        for (int r2 = 0; r2 < 16; ++r2) a16[r2] = (crow(r2, hi) <= l31) ? a16[r2] : -3.0e38f;
      }
#pragma unroll
      for (int r2 = 0; r2 < 16; r2 += 2) mx = fmaxf(fmaxf(mx, a16[r2]), a16[r2 + 1]);
    } else {
#pragma unroll
      for (int i = 0; i < 16; ++i) a16[i] = -3.0e38f;
    }
    sc[kt] = a16;
  }
  mx = fmaxf(mx, __shfl_xor(mx, 32));
  const float mxs = -mx * scale_l2;
  const f32x2 sc2 = {scale_l2, scale_l2}, mx2 = {mxs, mxs};
  f32x2 sum2 = {0, 0};
#pragma unroll
  for (int kt = 0; kt < 9; ++kt)
#pragma unroll
    for (int r = 0; r < 16; r += 2) {
      f32x2 t = {sc[kt][r], sc[kt][r + 1]};
      t = t * sc2 + mx2;
      f32x2 e = {__builtin_amdgcn_exp2f(t[0]), __builtin_amdgcn_exp2f(t[1])};
      sc[kt][r] = e[0]; sc[kt][r + 1] = e[1];
      sum2 += e;
    }
  float sum = sum2[0] + sum2[1];
  sum += __shfl_xor(sum, 32);
  f32x16 o = zero16;
  const int q4 = (lane & 15) >> 2, p4 = lane & 3, blk = (lane >> 4) & 1;
#pragma unroll
  for (int kt = 0; kt < 9; ++kt) {
#pragma unroll
    for (int s = 0; s < 2; ++s) {
      u32x4 pk;
#pragma unroll
      for (int j = 0; j < 4; ++j) pk[j] = pack2(sc[kt][8 * s + 2 * j], sc[kt][8 * s + 2 * j + 1]);
      bf16x8 pf = __builtin_bit_cast(bf16x8, pk);
      int r0 = wid * 32 + kt * 32 + 16 * s + 4 * hi + q4;
      const char* vp = sV + r0 * 64 + (16 * blk + 4 * p4) * 2;
      bf16x8 vf = cat8(tr_read(vp), tr_read(vp + 8 * 64));
      o = __builtin_amdgcn_mfma_f32_32x32x16_bf16(vf, pf, o, 0, 0, 0);
    }
  }
  float inv = 1.f / sum;
  u16* op = att + (long)qi * D_ + h * 32;
#pragma unroll
  for (int gq = 0; gq < 4; ++gq) {
    u32x2 w2;
    w2[0] = pack2(o[4 * gq + 0] * inv, o[4 * gq + 1] * inv);
    w2[1] = pack2(o[4 * gq + 2] * inv, o[4 * gq + 3] * inv);
    *(u32x2*)(op + 8 * gq + 4 * hi) = w2;
  }
}

using G2C = GemmCfg<64, 2, 2, 4, 4>;

__global__ __launch_bounds__(512, 2) void k_out(const u16* __restrict__ a, const u16* __restrict__ w,
                                                const u16* __restrict__ xold, u16* __restrict__ xnew,
                                                float2* __restrict__ stats) {
  __shared__ __attribute__((aligned(16))) char smem[G2C::LDS_BYTES];
  const int tid = threadIdx.x, lane = tid & 63;
  int bid = blockIdx.x;
  int xcd = bid & 7, idx = bid >> 3;
  int mblk = xcd * 4 + (idx & 3), nblk = idx >> 2;
  const int m0 = mblk * 64;
  f32x4 acc[G2C::MB][G2C::NB];
#pragma unroll
  for (int i = 0; i < G2C::MB; ++i)
#pragma unroll
    for (int j = 0; j < G2C::NB; ++j) acc[i][j] = (f32x4){0, 0, 0, 0};
  gemm_mainloop<64, 2, 2, 4, 4>([]() {}, a, D_, w, D_, m0, nblk * 128, 64, smem, acc);

  const int wid = tid >> 6, wm = wid / 4, wn = wid % 4;
  float* T = (float*)smem;
  constexpr int TP = 132;
  const int l15 = lane & 15, g = lane >> 4;
#pragma unroll
  for (int mb = 0; mb < G2C::MB; ++mb)
#pragma unroll
    for (int nb = 0; nb < G2C::NB; ++nb)
      *(f32x4*)(T + (wm * 32 + mb * 16 + l15) * TP + wn * 32 + nb * 16 + g * 4) = acc[mb][nb];
  __syncthreads();
  int row = tid >> 3, part = tid & 7;
  const u16* xo = xold + (long)(m0 + row) * D_ + nblk * 128 + part * 16;
  u16* xn = xnew + (long)(m0 + row) * D_ + nblk * 128 + part * 16;
  float v[16];
  float s = 0;
#pragma unroll
  for (int i = 0; i < 2; ++i) {
    bf16x8 xv = *(const bf16x8*)(xo + i * 8);
    u32x4 o;
#pragma unroll
    for (int j = 0; j < 4; ++j) {
      float f0 = bfr(bf2f((u16)xv[2 * j]) + T[row * TP + part * 16 + i * 8 + 2 * j]);
      float f1 = bfr(bf2f((u16)xv[2 * j + 1]) + T[row * TP + part * 16 + i * 8 + 2 * j + 1]);
      v[i * 8 + 2 * j] = f0; v[i * 8 + 2 * j + 1] = f1;
      s += f0 + f1;
      o[j] = pack2(f0, f1);
    }
    *(u32x4*)(xn + i * 8) = o;
  }
  s += __shfl_xor(s, 1); s += __shfl_xor(s, 2); s += __shfl_xor(s, 4);
  float mu = s * (1.f / 128.f), m2 = 0;
#pragma unroll
  for (int j = 0; j < 16; ++j) { float d = v[j] - mu; m2 += d * d; }
  m2 += __shfl_xor(m2, 1); m2 += __shfl_xor(m2, 2); m2 += __shfl_xor(m2, 4);
  if (part == 0) stats[(long)(m0 + row) * NPART + nblk] = make_float2(s, m2);
}

extern "C" void kernel_launch(void* const* d_in, const int* in_sizes, int n_in, void* d_out, int out_size,
                              void* d_ws, size_t ws_size, hipStream_t stream) {
  const u16* x_in = (const u16*)d_in[0];
  const u16* A_wqkv = (const u16*)d_in[1];
  const u16* A_wo = (const u16*)d_in[2];
  const u16* B_wqkv = (const u16*)d_in[4];
  const u16* B_wo = (const u16*)d_in[5];
  u16* out = (u16*)d_out;
  char* ws = (char*)d_ws;
  size_t off = 0;
  auto take = [&](size_t bytes) { char* p = ws + off; off += (bytes + 255) & ~(size_t)255; return p; };
  u16* xb0 = (u16*)take((size_t)S_ * D_ * 2);
  u16* xb1 = (u16*)take((size_t)S_ * D_ * 2);
  u16* qkv = (u16*)take((size_t)S_ * N3 * 2);
  u16* att = (u16*)take((size_t)S_ * D_ * 2);
  u16* Mm = (u16*)take((size_t)D_ * D_ * 2);
  float* kvp = (float*)take((size_t)16 * H_ * 1024 * 4);
  float* zp = (float*)take((size_t)16 * D_ * 4);
  float2* stats = (float2*)take((size_t)S_ * NPART * 8);
  float* csA = (float*)take((size_t)CSP * N3 * 4);
  float* csB = (float*)take((size_t)CSP * N3 * 4);

  unsigned* junk = (unsigned*)take((size_t)64 * 256 * 4);
  k_init<<<S_ / 4 + 2 * 12 * CSP + 64, 256, 0, stream>>>(x_in, stats, A_wqkv, B_wqkv, csA, csB, A_wo, B_wo, junk);
  k_qkv<0><<<256, 512, 0, stream>>>(x_in, A_wqkv, stats, csA, att, kvp, zp);
  k_lin_mid<<<384, 256, 0, stream>>>(kvp, zp, A_wo, Mm, att);
  k_out<<<256, 512, 0, stream>>>(att, Mm, x_in, xb0, stats);
  k_qkv<1><<<256, 512, 0, stream>>>(xb0, B_wqkv, stats, csB, qkv, kvp, zp);
  k_swa<<<512, 256, 0, stream>>>(qkv, att);
  k_out<<<256, 512, 0, stream>>>(att, B_wo, xb0, xb1, stats);
  k_qkv<0><<<256, 512, 0, stream>>>(xb1, A_wqkv, stats, csA, att, kvp, zp);
  k_lin_mid<<<384, 256, 0, stream>>>(kvp, zp, A_wo, Mm, att);
  k_out<<<256, 512, 0, stream>>>(att, Mm, xb1, xb0, stats);
  k_qkv<1><<<256, 512, 0, stream>>>(xb0, B_wqkv, stats, csB, qkv, kvp, zp);
  k_swa<<<512, 256, 0, stream>>>(qkv, att);
  k_out<<<256, 512, 0, stream>>>(att, B_wo, xb0, out, stats);
}
```
